```python
import torch

def _softmax(x, axis=-1):
    return torch.softmax(x, dim=axis)
def _gelu(x):
    return torch.nn.functional.gelu(x, approximate="tanh")
def _silu(x):
    return torch.nn.functional.silu(x)
def _sigmoid(x):
    return torch.sigmoid(x)
def _layernorm(x, axis=-1, eps=1e-5):
    m = x.mean(dim=axis, keepdim=True)
    v = x.var(dim=axis, keepdim=True, unbiased=False)
    return (x - m) / torch.sqrt(v + eps)
def _scatter_add(tgt, idx, src, axis=0):
    return tgt.clone().index_add_(axis, idx, src)
def _segment_sum(src, seg, n):
    out = torch.zeros((n,) + src.shape[1:], dtype=src.dtype)
    return out.index_add_(0, seg, src)
def _pad(x, pw):
    flat = tuple(v for pair in reversed(pw) for v in pair)
    return torch.nn.functional.pad(x, flat)

kernel_name = 'recurrent_scan__softmax_full_mha__dense_activation_squared_relu__prompt'

def setup_inputs():

    _w = (lambda a: (lambda t: t.to(torch.bfloat16) if t.is_floating_point() else t)(torch.as_tensor(a)))
    def _A_setup():
        B, S, D = 1, 2048, 512
        return {
            "x": _w((lambda *s: torch.randn(*s, dtype=torch.bfloat16) * 0.05)(B, S, D)),
            "wqkv": _w((lambda *s: torch.randn(*s, dtype=torch.bfloat16) * 0.05)(D, 3 * D)),
            "wo": _w((lambda *s: torch.randn(*s, dtype=torch.bfloat16) * 0.05)(D, D)),
        }
    _A_in = _A_setup()

    def _B_setup():
        B, S, D, F = 1, 2048, 512, 1792
        return {
            "x": _w((lambda *s: torch.randn(*s, dtype=torch.bfloat16) * 0.05)(B, S, D)),
            "w1": _w((lambda *s: torch.randn(*s, dtype=torch.bfloat16) * 0.05)(D, F)),
            "b1": _w((lambda *s: torch.randn(*s, dtype=torch.bfloat16) * 0.4)(F)),
            "w2": _w((lambda *s: torch.randn(*s, dtype=torch.bfloat16) * 0.05)(F, D)),
            "b2": _w((lambda *s: torch.randn(*s, dtype=torch.bfloat16) * 1.6)(D)),
        }
    _B_in = _B_setup()

    return {
        "A_x": _A_in["x"],
        "A_wqkv": _A_in["wqkv"],
        "A_wo": _A_in["wo"],
        "B_x": _B_in["x"],
        "B_w1": _B_in["w1"],
        "B_b1": _B_in["b1"],
        "B_w2": _B_in["w2"],
        "B_b2": _B_in["b2"],
    }

def reference(A_x, A_wqkv, A_wo, B_x, B_w1, B_b1, B_w2, B_b2):
    def _A_ref(x, wqkv, wo):
        B, S, D = x.shape
        H = 32; Dh = D // H
        qkv = (lambda x, *d: x.permute(*d))((x @ wqkv).reshape(B, S, 3, H, Dh), 2, 0, 3, 1, 4)
        q, k, v = qkv[0], qkv[1], qkv[2]
        scale = Dh ** -0.5
        scores = torch.einsum("bhqd,bhkd->bhqk", q, k) * scale
        p = _softmax(scores, axis=-1)
        o = torch.einsum("bhqk,bhkd->bhqd", p, v)
        return (lambda x, *d: x.permute(*d))(o, 0, 2, 1, 3).reshape(B, S, D) @ wo

    def _B_ref(x, w1, b1, w2, b2):
        h = torch.einsum("bsd,df->bsf", x, w1) + b1
        zero = torch.zeros_like(h)
        r = torch.where(h > zero, h, zero)
        h = r * r
        out = torch.einsum("bsf,fd->bsd", h, w2) + b2
        return out

    x = A_x
    xn = _layernorm(x, axis=-1)
    s = _A_ref(xn, A_wqkv, A_wo)
    g = _silu(_B_ref(xn, B_w1, B_b1, B_w2, B_b2))
    return x + s * g
```

```cpp
#include <hip/hip_runtime.h>
#include <hip/hip_bf16.h>
#include <stdint.h>

typedef __attribute__((ext_vector_type(8))) __bf16 bf16x8;
typedef __attribute__((ext_vector_type(16))) __bf16 bf16x16;
typedef __attribute__((ext_vector_type(2))) __bf16 bf16x2;
typedef __attribute__((ext_vector_type(4))) short s16x4;
typedef __attribute__((ext_vector_type(16))) float f32x16;
typedef __attribute__((ext_vector_type(8))) float f32x8;
typedef __attribute__((ext_vector_type(4))) float f32x4;
typedef __attribute__((ext_vector_type(2))) float f32x2;
typedef __attribute__((ext_vector_type(4))) unsigned u32x4;
typedef __attribute__((ext_vector_type(2))) unsigned u32x2;
#define LAS __attribute__((address_space(3)))
#define GAS __attribute__((address_space(1)))
#define DI __device__ __forceinline__
typedef LAS char* ldsp;

constexpr int SEQ = 2048, DM = 512, NH = 32, FF = 1792;
constexpr size_t WS_Q = 65536;
constexpr size_t WS_KV = WS_Q + 2097152;
constexpr size_t WS_H = WS_KV + 4194304;
constexpr size_t WS_O = WS_H + 64 * 112 * 1024;
constexpr size_t WS_W = WS_O + 2097152;
constexpr size_t WS_G = WS_W + 16 * 144 * 1024;
constexpr int LDS_BYTES = 163840;
constexpr int UNIT = 16384, MISC = 9 * UNIT;
constexpr int KVSTRIDE = 2064, QSLOT = 139264;
constexpr size_t WS_END = WS_G + 256 * 512 * 32;

#define MFMA32(a, b, c) __builtin_amdgcn_mfma_f32_32x32x16_bf16((a), (b), (c), 0, 0, 0)
#define SBAR() __builtin_amdgcn_sched_barrier(0)
#define PIN(x) asm volatile("" : "+v"(x))
#define WAIT_BAR(N) asm volatile("s_waitcnt vmcnt(%0) lgkmcnt(0)\n\ts_barrier" ::"i"(N) : "memory")

#define STAMP(i)
#define STAMP_DECL(n)
#define STAMP_FLUSH(k, n)
DI bf16x8 as_bf(u32x4 v) { return __builtin_bit_cast(bf16x8, v); }
DI unsigned pk2(float lo, float hi) { bf16x2 r = {(__bf16)lo, (__bf16)hi}; return __builtin_bit_cast(unsigned, r); }
DI float bf_lo(unsigned u) { return __uint_as_float(u << 16); }
DI float bf_hi(unsigned u) { return __uint_as_float(u & 0xffff0000u); }
DI float swap_add(float v) { auto r = __builtin_amdgcn_permlane32_swap(__float_as_uint(v), __float_as_uint(v), false, false); return __uint_as_float(r[0]) + __uint_as_float(r[1]); }
DI float swap_max(float v) { auto r = __builtin_amdgcn_permlane32_swap(__float_as_uint(v), __float_as_uint(v), false, false); return fmaxf(__uint_as_float(r[0]), __uint_as_float(r[1])); }

DI void glds2(const char* sbase, unsigned v0, unsigned v1, unsigned dst) {
  unsigned keep;
  asm volatile("s_mov_b32 %0, m0\n\ts_mov_b32 m0, %4\n\ts_nop 0\n\tglobal_load_lds_dwordx4 %1, %3\n\tglobal_load_lds_dwordx4 %2, %3 offset:1024\n\ts_mov_b32 m0, %0"
               : "=&s"(keep) : "v"(v0), "v"(v1), "s"(sbase), "s"(dst) : "memory");
}
DI void glds1(const char* sbase, unsigned v0, unsigned dst) {
  unsigned keep;
  asm volatile("s_mov_b32 %0, m0\n\ts_mov_b32 m0, %3\n\ts_nop 0\n\tglobal_load_lds_dwordx4 %1, %2\n\ts_mov_b32 m0, %0"
               : "=&s"(keep) : "v"(v0), "s"(sbase), "s"(dst) : "memory");
}
DI u32x4 lds_r128(ldsp p) { return *(LAS u32x4*)p; }
DI u32x4 lds_tr2(ldsp p0, ldsp p1) {
  s16x4 a = __builtin_amdgcn_ds_read_tr16_b64_v4i16((LAS s16x4*)p0);
  s16x4 b = __builtin_amdgcn_ds_read_tr16_b64_v4i16((LAS s16x4*)p1);
  u32x2 ua = __builtin_bit_cast(u32x2, a), ub = __builtin_bit_cast(u32x2, b);
  return u32x4{ua[0], ua[1], ub[0], ub[1]};
}
DI void acc_sum(float& s, u32x4 v) {
#pragma unroll
  for (int i = 0; i < 4; ++i) { s += bf_lo(v[i]); s += bf_hi(v[i]); }
}
DI void acc_stat(float& s, float& q, u32x4 v) {
#pragma unroll
  for (int i = 0; i < 4; ++i) { float a = bf_lo(v[i]), b = bf_hi(v[i]); s += a; q = fmaf(a, a, q); s += b; q = fmaf(b, b, q); }
}

DI void phase12(const char* x, const char* wqkv, const char* wo, const char* w1, const char* b1, const char* w2, char* ws, ldsp lds) {
  const int tid = threadIdx.x, l = tid & 63;
  const int w = __builtin_amdgcn_readfirstlane(tid >> 6), m = w & 3, g2 = w >> 2;
  const int xcd = blockIdx.x & 7, jj = blockIdx.x >> 3;
  const int cb = (xcd & 3) * 4 + (jj & 3), rb = (xcd >> 2) * 8 + (jj >> 2);
  const int rot = (rb + cb) & 7;
  STAMP_DECL(10); STAMP(0);
  const bool tr_role = cb >= 14;
  const int z = rb * 2 + (cb - 14);
  const unsigned lds0 = (unsigned)(uintptr_t)lds;
  const int t = l & 31, h = l >> 5;
  const unsigned vx0 = (16 * w + (l >> 3)) * 1024 + (((l & 7) ^ (l >> 4)) << 4);
  const unsigned vx1 = (16 * w + 8 + (l >> 3)) * 1024 + (((l & 7) ^ (4 + (l >> 4))) << 4) - 1024;
  const unsigned cswz = ((l & 15) ^ ((l >> 4) << 2)) << 4;
  const unsigned vq0 = (8 * w + (l >> 4)) * 3072 + cswz, vq1 = vq0 + 4 * 3072 - 1024;
  const unsigned st2 = tr_role ? 1024 : 3584;
  const unsigned vw0 = (8 * w + (l >> 4)) * st2 + cswz, vw1 = vw0 + 4 * st2 - 1024;
  const int colstart = (96 * cb) & ~63;
  const char* xs = x + (size_t)rb * 128 * 1024;
  const char* qs = wqkv + colstart * 2;
  auto issue = [&](int n) {
    const unsigned dst = lds0 + (n % 9) * UNIT + w * 2048;
    if (n < 16) {
      const int kc = ((n >> 1) + rot) & 7;
      if (n & 1) glds2(qs + kc * 64 * 3072, vq0, vq1, dst); else glds2(xs + kc * 128, vx0, vx1, dst);
    } else {
      const int s = n - 16;
      int kc = (z >> 2) + 8 * s; kc = kc < 36 ? kc : 0;
      const char* pt = kc < 28 ? w2 : wo - 28 * 65536;
      const int offn = ((s + rot) & 7) * 64 * 3584 + cb * 256, offt = kc * 65536 + (z & 3) * 256;
      const char* src = (tr_role ? pt : w1) + (tr_role ? offt : offn);
      glds2(src, vw0, vw1, dst);
    }
  };
  if (w == 0 && !tr_role) {
    unsigned keep;
    asm volatile("s_mov_b32 %0, m0\n\ts_mov_b32 m0, %3\n\ts_nop 0\n\tglobal_load_lds_dword %1, %2\n\ts_mov_b32 m0, %0"
                 : "=&s"(keep) : "v"(l * 4), "s"(b1 + cb * 256), "s"(lds0 + MISC + 10240) : "memory");
  }
#pragma unroll
  for (int n = 0; n < 9; ++n) issue(n);
  const int mu = m ^ (2 * g2);
  const unsigned xb = (32 * m + t) * 128 + ((h ^ ((t >> 1) & 7)) << 4);
  const int q4 = (l >> 2) & 3;
  unsigned xa[4], wa[2][4];
#pragma unroll
  for (int i = 0; i < 4; ++i) {
    xa[i] = xb ^ ((i ^ mu) << 5);
#pragma unroll
    for (int j = 0; j < 2; ++j) wa[j][i] = h * 2048 + q4 * 256 + (((2 * g2 + j) ^ q4) << 6) + ((l >> 4) & 1) * 32 + (l & 3) * 8 + ((i ^ mu) << 12);
  }
  const int tau0 = (colstart >> 5) + 2 * g2;
  u32x4 xf[8][4];
  f32x16 acc0 = {}, acc1 = {};
  float rsum = 0.f, rsq = 0.f, cs0 = 0.f, cs1 = 0.f;
#pragma unroll
  for (int s = 0; s < 8; ++s) {
    if (s == 0) WAIT_BAR(14); else WAIT_BAR(10);
    if (s == 0) STAMP(1); if (s == 1) STAMP(2); if (s == 4) STAMP(3);
    if (s >= 1) { issue(2 * s + 7); issue(2 * s + 8); }
    ldsp ux = lds + ((2 * s) % 9) * UNIT, uw = lds + ((2 * s + 1) % 9) * UNIT;
    u32x4 wf0[4], wf1[4];
#pragma unroll
    for (int i = 0; i < 4; ++i) {
      xf[s][i] = lds_r128(ux + xa[i]);
      wf0[i] = lds_tr2(uw + wa[0][i], uw + wa[0][i] + 1024);
      wf1[i] = lds_tr2(uw + wa[1][i], uw + wa[1][i] + 1024);
    }
#pragma unroll
    for (int i = 0; i < 4; ++i) {
      acc0 = MFMA32(as_bf(wf0[i]), as_bf(xf[s][i]), acc0);
      acc1 = MFMA32(as_bf(wf1[i]), as_bf(xf[s][i]), acc1);
    }
    acc_stat(rsum, rsq, xf[s][0]); acc_sum(cs0, wf0[0]);
    acc_stat(rsum, rsq, xf[s][1]); acc_sum(cs1, wf1[0]);
    PIN(rsum); PIN(rsq); PIN(cs0); PIN(cs1);
  }
  LAS float* rowp = (LAS float*)(lds + MISC);
  LAS float* colp = (LAS float*)(lds + MISC + 2048);
  LAS float* colp2 = (LAS float*)(lds + MISC + 4096);
  LAS float* priv = (LAS float*)(lds + MISC + 6144 + w * 512);
  {
    rsum = swap_add(rsum); rsq = swap_add(rsq); cs0 = swap_add(cs0); cs1 = swap_add(cs1);
    if (h == 0) *(LAS f32x2*)(rowp + (g2 * 128 + 32 * m + t) * 2) = f32x2{rsum, rsq};
    colp[m * 128 + (2 * g2 + h) * 32 + t] = h ? cs1 : cs0;
  }
  WAIT_BAR(12);
  STAMP(4);
  issue(23);
  float rstd, mur;
  {
    f32x2 a = *(LAS f32x2*)(rowp + (32 * m + t) * 2), b = *(LAS f32x2*)(rowp + (128 + 32 * m + t) * 2);
    const float mean = (a[0] + b[0]) * (1.f / 512.f);
    const float var = fmaxf((a[1] + b[1]) * (1.f / 512.f) - mean * mean, 0.f);
    rstd = 1.0f / sqrtf(var + 1e-5f); mur = mean * rstd;
    const int ci = (2 * g2 + h) * 32 + t;
    const float c = colp[ci] + colp[128 + ci] + colp[256 + ci] + colp[384 + ci];
    priv[l] = c;
  }
  const int ttile = rb * 4 + m;
#pragma unroll
  for (int j = 0; j < 2; ++j) {
    const int tau = tau0 + j;
    const f32x16& acc = j ? acc1 : acc0;
    if (tau >= 3 * cb && tau < 3 * cb + 3) {
      const int hp = tau & 15;
      float y[16];
      const float sc = tau < 16 ? 0.25f * 1.4426950408889634f : 1.0f;
      const float rs = rstd * sc, mm = -mur * sc;
#pragma unroll
      for (int g = 0; g < 4; ++g) {
        f32x4 c = *(LAS f32x4*)(priv + j * 32 + 8 * g + 4 * h);
#pragma unroll
        for (int e = 0; e < 4; ++e) y[4 * g + e] = fmaf(acc[4 * g + e], rs, mm * c[e]);
      }
      u32x4 o0 = {pk2(y[0], y[1]), pk2(y[2], y[3]), pk2(y[4], y[5]), pk2(y[6], y[7])};
      u32x4 o1 = {pk2(y[8], y[9]), pk2(y[10], y[11]), pk2(y[12], y[13]), pk2(y[14], y[15])};
      unsigned doff = tau < 16 ? WS_Q + ((2 * hp) * 64 + ttile) * 1024 + l * 16 : WS_KV + ((2 * hp) * 64 + ttile) * 2048 + l * 16;
      unsigned dstep = tau < 16 ? 65536 : 131072;
      if (tau >= 32) {
        ldsp vs = lds + 6 * UNIT + w * 2048;
        *(LAS u32x4*)(vs + t * 32 + h * 16) = o0;
        *(LAS u32x4*)(vs + 1024 + t * 32 + h * 16) = o1;
        ldsp rp = vs + ((l >> 4) & 1) * 1024 + (8 * h + q4) * 32 + (l & 1) * 16 + ((l >> 1) & 1) * 8;
        o0 = lds_tr2(rp, rp + 128);
        o1 = lds_tr2(rp + 512, rp + 640);
        doff = WS_KV + ((2 * hp + (t >> 4)) * 64 + ttile) * 2048 + 1024 + h * 256 + (t & 15) * 16;
        dstep = 512;
      }
      *(u32x4*)(ws + doff) = o0;
      *(u32x4*)(ws + doff + dstep) = o1;
    }
  }
  STAMP(5);
  if (!tr_role) {
    acc0 = f32x16{}; acc1 = f32x16{}; cs0 = 0.f; cs1 = 0.f;
#pragma unroll
    for (int s = 0; s < 8; ++s) {
      if (s >= 1) WAIT_BAR(2 * (7 - s) + 2);
      ldsp uw = lds + ((16 + s) % 9) * UNIT;
      u32x4 wf0[4], wf1[4];
#pragma unroll
      for (int i = 0; i < 4; ++i) {
        wf0[i] = lds_tr2(uw + wa[0][i], uw + wa[0][i] + 1024);
        wf1[i] = lds_tr2(uw + wa[1][i], uw + wa[1][i] + 1024);
      }
#pragma unroll
      for (int i = 0; i < 4; ++i) {
        acc0 = MFMA32(as_bf(wf0[i]), as_bf(xf[s][i]), acc0);
        acc1 = MFMA32(as_bf(wf1[i]), as_bf(xf[s][i]), acc1);
      }
      acc_sum(cs0, wf0[0]); acc_sum(cs1, wf1[0]);
      PIN(cs0); PIN(cs1);
    }
    cs0 = swap_add(cs0); cs1 = swap_add(cs1);
    colp2[m * 128 + (2 * g2 + h) * 32 + t] = h ? cs1 : cs0;
    WAIT_BAR(63);
    STAMP(6);
    {
      const int ci = (2 * g2 + h) * 32 + t;
      priv[l] = colp2[ci] + colp2[128 + ci] + colp2[256 + ci] + colp2[384 + ci];
      priv[64 + l] = bf_lo(*(LAS unsigned short*)(lds + MISC + 10240 + ci * 2));
    }
#pragma unroll
    for (int j = 0; j < 2; ++j) {
      const f32x16& acc = j ? acc1 : acc0;
      float y[16];
#pragma unroll
      for (int g = 0; g < 4; ++g) {
        f32x4 c = *(LAS f32x4*)(priv + j * 32 + 8 * g + 4 * h), bb = *(LAS f32x4*)(priv + 64 + j * 32 + 8 * g + 4 * h);
#pragma unroll
        for (int e = 0; e < 4; ++e) { float v = fmaxf(fmaf(acc[4 * g + e], rstd, fmaf(-mur, c[e], bb[e])), 0.f); y[4 * g + e] = v * v; }
      }
      char* dst = ws + WS_H + (size_t)(ttile * 112 + cb * 8 + (2 * g2 + j) * 2) * 1024 + l * 16;
#pragma unroll
      for (int s = 0; s < 2; ++s)
        *(u32x4*)(dst + s * 1024) = u32x4{pk2(y[8 * s], y[8 * s + 1]), pk2(y[8 * s + 2], y[8 * s + 3]), pk2(y[8 * s + 4], y[8 * s + 5]), pk2(y[8 * s + 6], y[8 * s + 7])};
    }
  } else {
#pragma unroll
    for (int s = 0; s < 5; ++s) {
      if (s >= 1) WAIT_BAR(2 * (7 - s));
      const int u = z + 32 * s;
      if (u < 144) {
        ldsp uw = lds + ((16 + s) % 9) * UNIT;
#pragma unroll
        for (int j = 0; j < 2; ++j) {
          const unsigned a = m * 4096 + h * 1024 + q4 * 256 + (((2 * g2 + j) ^ q4) << 6) + ((l >> 4) & 1) * 32 + (l & 3) * 8;
          u32x4 f = lds_tr2(uw + a, uw + a + 2048);
          *(u32x4*)(ws + WS_W + (size_t)(((u & 3) * 4 + 2 * g2 + j) * 144 + (u >> 2) * 4 + m) * 1024 + l * 16) = f;
        }
      }
    }
    asm volatile("s_waitcnt vmcnt(0)" ::: "memory");
  }
  STAMP(7);
  STAMP(8);
  STAMP_FLUSH(0, 10);
}

DI f32x16 smf(u32x4 a, f32x8 p, f32x16 c, int idx) {
  return __builtin_amdgcn_smfmac_f32_32x32x32_bf16(as_bf(a), __builtin_bit_cast(bf16x16, p), c, idx, 0, 0);
}
DI f32x8 lo8(const f32x16& v) { return __builtin_shufflevector(v, v, 0, 1, 2, 3, 4, 5, 6, 7); }
DI f32x8 hi8(const f32x16& v) { return __builtin_shufflevector(v, v, 8, 9, 10, 11, 12, 13, 14, 15); }
DI float max16(const f32x16& s) {
  float a = fmaxf(fmaxf(s[0], s[1]), s[2]), b = fmaxf(fmaxf(s[3], s[4]), s[5]);
  a = fmaxf(fmaxf(a, s[6]), s[7]); b = fmaxf(fmaxf(b, s[8]), s[9]); a = fmaxf(fmaxf(a, s[10]), s[11]); b = fmaxf(fmaxf(b, s[12]), s[13]);
  a = fmaxf(fmaxf(a, s[14]), s[15]);
  return swap_max(fmaxf(a, b));
}

DI void phase3(const char* b2, char* ws, ldsp lds, float (&gout)[8]) {
  const int tid = threadIdx.x, l = tid & 63;
  const int w = __builtin_amdgcn_readfirstlane(tid >> 6);
  const int xcd = blockIdx.x & 7, jj = blockIdx.x >> 3;
  const int head = xcd * 4 + (jj >> 3), qt = (jj & 7) * 8 + w;
  const int tb = xcd * 4 + (jj >> 3), nb = jj & 7;
  const unsigned lds0 = (unsigned)(uintptr_t)lds;
  const int t = l & 31, h = l >> 5;
  STAMP_DECL(10); STAMP(0);
  {
    glds1(ws + WS_Q + (size_t)(head * 64 + qt) * 1024, l * 16, lds0 + QSLOT + w * 1024);
    const char* src = ws + WS_KV + (size_t)head * 131072 + w * 2048;
#pragma unroll
    for (int c = 0; c < 8; ++c) glds2(src + c * 16384, l * 16, l * 16, lds0 + (c * 8 + w) * KVSTRIDE);
  }
  if (w == 0) *(LAS u32x4*)(lds + l * KVSTRIDE + 2048) = u32x4{0x3f803f80u, 0x3f803f80u, 0x3f803f80u, 0x3f803f80u};
  const unsigned ka = lds0 + l * 16;
  const unsigned vreal = 1024 + h * 256 + (t & 15) * 16;
  const bool ones = (t >= 16 && t < 20) || t >= 28;
  const unsigned va0 = lds0 + (t < 16 ? vreal : (ones ? 2048u : 1024u + h * 256));
  const unsigned va1 = lds0 + (t < 16 ? vreal + 512 : (ones ? 2048u : 1536u + h * 256));
  int idx = 0xDDDD; PIN(idx);
  const unsigned oh0 = WS_H + (2 * tb * 112 + 14 * w) * 1024 + l * 16, ow0 = WS_W + (2 * nb * 144 + 14 * w) * 1024 + l * 16;
  f32x16 O, F00, F01, F10, F11;
  STAMP(1);
  {
    unsigned so, cnt, ph;
    asm volatile(
      "s_mov_b64 s[60:61], %[ws]\n\t"
      "s_mov_b32 s62, %[wsz]\n\t"
      "s_mov_b32 s63, 0x20000\n\t"
      "s_mov_b32 %[so], 0\n\t"
      "v_mov_b32 v128, %[ka]\n\t"
      "v_mov_b32 v129, %[va0]\n\t"
      "v_mov_b32 v130, %[va1]\n\t"
      "v_mov_b32 v88, 0\n\t"
      "v_mov_b32 v89, 0\n\t"
      "v_mov_b32 v90, 0\n\t"
      "v_mov_b32 v91, 0\n\t"
      "s_nop 1\n\t"
      "v_mfma_f32_32x32x16_bf16 %[O], v[88:91], v[88:91], 0\n\t"
      "v_mfma_f32_32x32x16_bf16 v[48:63], v[88:91], v[88:91], 0\n\t"
      "v_mfma_f32_32x32x16_bf16 %[F00], v[88:91], v[88:91], 0\n\t"
      "v_mfma_f32_32x32x16_bf16 %[F01], v[88:91], v[88:91], 0\n\t"
      "v_mfma_f32_32x32x16_bf16 %[F10], v[88:91], v[88:91], 0\n\t"
      "v_mfma_f32_32x32x16_bf16 %[F11], v[88:91], v[88:91], 0\n\t"
      "s_waitcnt vmcnt(14) lgkmcnt(0)\n\t"
      "s_barrier\n\t"
      "ds_read_b128 v[92:95], %[qa]\n\t"
      "ds_read_b128 v[80:83], v128\n\t"
      "s_waitcnt lgkmcnt(0)\n\t"
      "v_mfma_f32_32x32x16_bf16 v[32:47], v[80:83], v[92:95], 0\n\t"
      "s_nop 15\n\t"
      "v_max3_f32 v131, v32, v33, v34\n\t"
      "v_max3_f32 v132, v35, v36, v37\n\t"
      "v_max3_f32 v131, v131, v38, v39\n\t"
      "v_max3_f32 v132, v132, v40, v41\n\t"
      "v_max3_f32 v131, v131, v42, v43\n\t"
      "v_max3_f32 v132, v132, v44, v45\n\t"
      "v_max3_f32 v131, v131, v46, v47\n\t"
      "v_max_f32 v131, v131, v132\n\t"
      "v_mov_b32 v132, v131\n\t"
      "s_nop 1\n\t"
      "v_permlane32_swap_b32 v131, v132\n\t"
      "s_nop 1\n\t"
      "v_max_f32 v131, v131, v132\n\t"
      "v_sub_f32 v64, 0, v131\n\t"
      "v_sub_f32 v65, 0, v131\n\t"
      "v_mov_b64 v[66:67], v[64:65]\n\t"
      "v_mov_b64 v[68:69], v[64:65]\n\t"
      "v_mov_b64 v[70:71], v[64:65]\n\t"
      "v_mov_b64 v[72:73], v[64:65]\n\t"
      "v_mov_b64 v[74:75], v[64:65]\n\t"
      "v_mov_b64 v[76:77], v[64:65]\n\t"
      "v_mov_b64 v[78:79], v[64:65]\n\t"
      "s_nop 1\n\t"
      "v_mfma_f32_32x32x16_bf16 v[32:47], v[80:83], v[92:95], v[64:79]\n\t"
      "ds_read_b128 v[80:83], v128 offset:2064\n\t"
      "ds_read_b128 v[84:87], v129\n\t"
      "s_nop 12\n\t"
      "s_mov_b32 %[cnt], 2\n\t"
      "s_mov_b32 %[ph], 0\n\t"
      "1:\n\t"
      "v_exp_f32 v32, v32\n\t"
      "v_exp_f32 v33, v33\n\t"
      "v_exp_f32 v34, v34\n\t"
      "s_waitcnt lgkmcnt(2)\n\t"
      "v_smfmac_f32_32x32x32_bf16 %[O], v[88:91], v[56:63], %[idx]\n\t"
      "ds_read_b128 v[88:91], v130 offset:0\n\t"
      "v_exp_f32 v35, v35\n\t"
      "v_exp_f32 v36, v36\n\t"
      "v_exp_f32 v37, v37\n\t"
      "v_exp_f32 v38, v38\n\t"
      "s_waitcnt lgkmcnt(2)\n\t"
      "v_mfma_f32_32x32x16_bf16 v[48:63], v[80:83], v[92:95], v[64:79]\n\t"
      "ds_read_b128 v[80:83], v128 offset:4128\n\t"
      "v_exp_f32 v39, v39\n\t"
      "v_exp_f32 v40, v40\n\t"
      "v_exp_f32 v41, v41\n\t"
      "v_exp_f32 v42, v42\n\t"
      "s_waitcnt lgkmcnt(2)\n\t"
      "v_smfmac_f32_32x32x32_bf16 %[O], v[84:87], v[32:39], %[idx]\n\t"
      "ds_read_b128 v[84:87], v129 offset:2064\n\t"
      "v_exp_f32 v43, v43\n\t"
      "v_exp_f32 v44, v44\n\t"
      "v_exp_f32 v45, v45\n\t"
      "s_sub_u32 %[cnt], %[cnt], 1\n\t"
      "v_exp_f32 v46, v46\n\t"
      "v_exp_f32 v47, v47\n\t"
      "v_exp_f32 v48, v48\n\t"
      "v_exp_f32 v49, v49\n\t"
      "v_exp_f32 v50, v50\n\t"
      "s_waitcnt lgkmcnt(2)\n\t"
      "v_smfmac_f32_32x32x32_bf16 %[O], v[88:91], v[40:47], %[idx]\n\t"
      "ds_read_b128 v[88:91], v130 offset:2064\n\t"
      "v_exp_f32 v51, v51\n\t"
      "v_exp_f32 v52, v52\n\t"
      "v_exp_f32 v53, v53\n\t"
      "v_exp_f32 v54, v54\n\t"
      "s_waitcnt lgkmcnt(2)\n\t"
      "v_mfma_f32_32x32x16_bf16 v[32:47], v[80:83], v[92:95], v[64:79]\n\t"
      "ds_read_b128 v[80:83], v128 offset:6192\n\t"
      "v_exp_f32 v55, v55\n\t"
      "v_exp_f32 v56, v56\n\t"
      "v_exp_f32 v57, v57\n\t"
      "v_exp_f32 v58, v58\n\t"
      "s_waitcnt lgkmcnt(2)\n\t"
      "v_smfmac_f32_32x32x32_bf16 %[O], v[84:87], v[48:55], %[idx]\n\t"
      "ds_read_b128 v[84:87], v129 offset:4128\n\t"
      "v_exp_f32 v59, v59\n\t"
      "v_exp_f32 v60, v60\n\t"
      "v_exp_f32 v61, v61\n\t"
      "v_add_u32 v128, 4128, v128\n\t"
      "v_add_u32 v129, 4128, v129\n\t"
      "v_add_u32 v130, 4128, v130\n\t"
      "s_cmp_lg_u32 %[cnt], 0\n\t"
      "v_exp_f32 v62, v62\n\t"
      "v_exp_f32 v63, v63\n\t"
      "s_cbranch_scc1 1b\n\t"
      "s_cmp_lg_u32 %[ph], 0\n\t"
      "s_cbranch_scc1 2f\n\t"
      "s_waitcnt vmcnt(0)\n\t"
      "s_barrier\n\t"
      "buffer_load_dwordx4 v[96:99], %[oh0], s[60:63], %[so] offen offset:0\n\t"
      "buffer_load_dwordx4 v[108:111], %[ow1], s[60:63], %[so] offen offset:0\n\t"
      "buffer_load_dwordx4 v[104:107], %[ow0], s[60:63], %[so] offen offset:0\n\t"
      "buffer_load_dwordx4 v[100:103], %[oh1], s[60:63], %[so] offen offset:0\n\t"
      "buffer_load_dwordx4 v[112:115], %[oh0], s[60:63], %[so] offen offset:1024\n\t"
      "buffer_load_dwordx4 v[124:127], %[ow1], s[60:63], %[so] offen offset:1024\n\t"
      "buffer_load_dwordx4 v[120:123], %[ow0], s[60:63], %[so] offen offset:1024\n\t"
      "buffer_load_dwordx4 v[116:119], %[oh1], s[60:63], %[so] offen offset:1024\n\t"
      "s_mov_b32 %[cnt], 2\n\t"
      "s_mov_b32 %[ph], 1\n\t"
      "s_branch 1b\n\t"
      "2:\n\t"
      "s_mov_b32 %[cnt], 7\n\t"
      "3:\n\t"
      "v_exp_f32 v32, v32\n\t"
      "v_exp_f32 v33, v33\n\t"
      "v_exp_f32 v34, v34\n\t"
      "s_waitcnt lgkmcnt(2)\n\t"
      "v_smfmac_f32_32x32x32_bf16 %[O], v[88:91], v[56:63], %[idx]\n\t"
      "ds_read_b128 v[88:91], v130 offset:0\n\t"
      "v_exp_f32 v35, v35\n\t"
      "v_exp_f32 v36, v36\n\t"
      "v_exp_f32 v37, v37\n\t"
      "v_exp_f32 v38, v38\n\t"
      "s_waitcnt lgkmcnt(2)\n\t"
      "v_mfma_f32_32x32x16_bf16 v[48:63], v[80:83], v[92:95], v[64:79]\n\t"
      "ds_read_b128 v[80:83], v128 offset:4128\n\t"
      "v_exp_f32 v39, v39\n\t"
      "v_exp_f32 v40, v40\n\t"
      "v_exp_f32 v41, v41\n\t"
      "v_exp_f32 v42, v42\n\t"
      "s_waitcnt lgkmcnt(2)\n\t"
      "v_smfmac_f32_32x32x32_bf16 %[O], v[84:87], v[32:39], %[idx]\n\t"
      "ds_read_b128 v[84:87], v129 offset:2064\n\t"
      "v_exp_f32 v43, v43\n\t"
      "v_exp_f32 v44, v44\n\t"
      "v_exp_f32 v45, v45\n\t"
      "s_add_u32 %[so], %[so], 2048\n\t"
      "s_cmp_eq_u32 %[so], 14336\n\t"
      "s_cselect_b32 s62, 0, s62\n\t"
      "s_waitcnt vmcnt(5)\n\t"
      "v_mfma_f32_32x32x16_bf16 %[F00], v[104:107], v[96:99], %[F00]\n\t"
      "v_exp_f32 v46, v46\n\t"
      "v_exp_f32 v47, v47\n\t"
      "v_exp_f32 v48, v48\n\t"
      "v_exp_f32 v49, v49\n\t"
      "v_exp_f32 v50, v50\n\t"
      "s_waitcnt lgkmcnt(2)\n\t"
      "v_smfmac_f32_32x32x32_bf16 %[O], v[88:91], v[40:47], %[idx]\n\t"
      "ds_read_b128 v[88:91], v130 offset:2064\n\t"
      "v_exp_f32 v51, v51\n\t"
      "v_exp_f32 v52, v52\n\t"
      "v_exp_f32 v53, v53\n\t"
      "v_exp_f32 v54, v54\n\t"
      "s_waitcnt lgkmcnt(2)\n\t"
      "v_mfma_f32_32x32x16_bf16 v[32:47], v[80:83], v[92:95], v[64:79]\n\t"
      "ds_read_b128 v[80:83], v128 offset:6192\n\t"
      "v_exp_f32 v55, v55\n\t"
      "v_exp_f32 v56, v56\n\t"
      "v_exp_f32 v57, v57\n\t"
      "v_exp_f32 v58, v58\n\t"
      "s_waitcnt lgkmcnt(2)\n\t"
      "v_smfmac_f32_32x32x32_bf16 %[O], v[84:87], v[48:55], %[idx]\n\t"
      "ds_read_b128 v[84:87], v129 offset:4128\n\t"
      "v_exp_f32 v59, v59\n\t"
      "v_exp_f32 v60, v60\n\t"
      "v_exp_f32 v61, v61\n\t"
      "v_mfma_f32_32x32x16_bf16 %[F01], v[108:111], v[96:99], %[F01]\n\t"
      "buffer_load_dwordx4 v[96:99], %[oh0], s[60:63], %[so] offen offset:0\n\t"
      "v_exp_f32 v62, v62\n\t"
      "v_exp_f32 v63, v63\n\t"
      "v_exp_f32 v32, v32\n\t"
      "v_exp_f32 v33, v33\n\t"
      "v_exp_f32 v34, v34\n\t"
      "s_waitcnt lgkmcnt(2)\n\t"
      "v_smfmac_f32_32x32x32_bf16 %[O], v[88:91], v[56:63], %[idx]\n\t"
      "ds_read_b128 v[88:91], v130 offset:4128\n\t"
      "v_exp_f32 v35, v35\n\t"
      "v_exp_f32 v36, v36\n\t"
      "v_exp_f32 v37, v37\n\t"
      "v_exp_f32 v38, v38\n\t"
      "s_waitcnt lgkmcnt(2)\n\t"
      "v_mfma_f32_32x32x16_bf16 v[48:63], v[80:83], v[92:95], v[64:79]\n\t"
      "ds_read_b128 v[80:83], v128 offset:8256\n\t"
      "v_exp_f32 v39, v39\n\t"
      "v_exp_f32 v40, v40\n\t"
      "v_exp_f32 v41, v41\n\t"
      "v_exp_f32 v42, v42\n\t"
      "s_waitcnt lgkmcnt(2)\n\t"
      "v_smfmac_f32_32x32x32_bf16 %[O], v[84:87], v[32:39], %[idx]\n\t"
      "ds_read_b128 v[84:87], v129 offset:6192\n\t"
      "v_exp_f32 v43, v43\n\t"
      "v_exp_f32 v44, v44\n\t"
      "v_exp_f32 v45, v45\n\t"
      "s_waitcnt vmcnt(5)\n\t"
      "v_mfma_f32_32x32x16_bf16 %[F11], v[108:111], v[100:103], %[F11]\n\t"
      "buffer_load_dwordx4 v[108:111], %[ow1], s[60:63], %[so] offen offset:0\n\t"
      "v_exp_f32 v46, v46\n\t"
      "v_exp_f32 v47, v47\n\t"
      "v_exp_f32 v48, v48\n\t"
      "v_exp_f32 v49, v49\n\t"
      "v_exp_f32 v50, v50\n\t"
      "s_waitcnt lgkmcnt(2)\n\t"
      "v_smfmac_f32_32x32x32_bf16 %[O], v[88:91], v[40:47], %[idx]\n\t"
      "ds_read_b128 v[88:91], v130 offset:6192\n\t"
      "v_exp_f32 v51, v51\n\t"
      "v_exp_f32 v52, v52\n\t"
      "v_exp_f32 v53, v53\n\t"
      "v_exp_f32 v54, v54\n\t"
      "s_waitcnt lgkmcnt(2)\n\t"
      "v_mfma_f32_32x32x16_bf16 v[32:47], v[80:83], v[92:95], v[64:79]\n\t"
      "ds_read_b128 v[80:83], v128 offset:10320\n\t"
      "v_exp_f32 v55, v55\n\t"
      "v_exp_f32 v56, v56\n\t"
      "v_exp_f32 v57, v57\n\t"
      "v_exp_f32 v58, v58\n\t"
      "s_waitcnt lgkmcnt(2)\n\t"
      "v_smfmac_f32_32x32x32_bf16 %[O], v[84:87], v[48:55], %[idx]\n\t"
      "ds_read_b128 v[84:87], v129 offset:8256\n\t"
      "v_exp_f32 v59, v59\n\t"
      "v_exp_f32 v60, v60\n\t"
      "v_exp_f32 v61, v61\n\t"
      "v_mfma_f32_32x32x16_bf16 %[F10], v[104:107], v[100:103], %[F10]\n\t"
      "buffer_load_dwordx4 v[104:107], %[ow0], s[60:63], %[so] offen offset:0\n\t"
      "buffer_load_dwordx4 v[100:103], %[oh1], s[60:63], %[so] offen offset:0\n\t"
      "v_exp_f32 v62, v62\n\t"
      "v_exp_f32 v63, v63\n\t"
      "v_exp_f32 v32, v32\n\t"
      "v_exp_f32 v33, v33\n\t"
      "v_exp_f32 v34, v34\n\t"
      "s_waitcnt lgkmcnt(2)\n\t"
      "v_smfmac_f32_32x32x32_bf16 %[O], v[88:91], v[56:63], %[idx]\n\t"
      "ds_read_b128 v[88:91], v130 offset:8256\n\t"
      "v_exp_f32 v35, v35\n\t"
      "v_exp_f32 v36, v36\n\t"
      "v_exp_f32 v37, v37\n\t"
      "v_exp_f32 v38, v38\n\t"
      "s_waitcnt lgkmcnt(2)\n\t"
      "v_mfma_f32_32x32x16_bf16 v[48:63], v[80:83], v[92:95], v[64:79]\n\t"
      "ds_read_b128 v[80:83], v128 offset:12384\n\t"
      "v_exp_f32 v39, v39\n\t"
      "v_exp_f32 v40, v40\n\t"
      "v_exp_f32 v41, v41\n\t"
      "v_exp_f32 v42, v42\n\t"
      "s_waitcnt lgkmcnt(2)\n\t"
      "v_smfmac_f32_32x32x32_bf16 %[O], v[84:87], v[32:39], %[idx]\n\t"
      "ds_read_b128 v[84:87], v129 offset:10320\n\t"
      "v_exp_f32 v43, v43\n\t"
      "v_exp_f32 v44, v44\n\t"
      "v_exp_f32 v45, v45\n\t"
      "s_waitcnt vmcnt(5)\n\t"
      "v_mfma_f32_32x32x16_bf16 %[F00], v[120:123], v[112:115], %[F00]\n\t"
      "v_exp_f32 v46, v46\n\t"
      "v_exp_f32 v47, v47\n\t"
      "v_exp_f32 v48, v48\n\t"
      "v_exp_f32 v49, v49\n\t"
      "v_exp_f32 v50, v50\n\t"
      "s_waitcnt lgkmcnt(2)\n\t"
      "v_smfmac_f32_32x32x32_bf16 %[O], v[88:91], v[40:47], %[idx]\n\t"
      "ds_read_b128 v[88:91], v130 offset:10320\n\t"
      "v_exp_f32 v51, v51\n\t"
      "v_exp_f32 v52, v52\n\t"
      "v_exp_f32 v53, v53\n\t"
      "v_exp_f32 v54, v54\n\t"
      "s_waitcnt lgkmcnt(2)\n\t"
      "v_mfma_f32_32x32x16_bf16 v[32:47], v[80:83], v[92:95], v[64:79]\n\t"
      "ds_read_b128 v[80:83], v128 offset:14448\n\t"
      "v_exp_f32 v55, v55\n\t"
      "v_exp_f32 v56, v56\n\t"
      "v_exp_f32 v57, v57\n\t"
      "v_exp_f32 v58, v58\n\t"
      "s_waitcnt lgkmcnt(2)\n\t"
      "v_smfmac_f32_32x32x32_bf16 %[O], v[84:87], v[48:55], %[idx]\n\t"
      "ds_read_b128 v[84:87], v129 offset:12384\n\t"
      "v_exp_f32 v59, v59\n\t"
      "v_exp_f32 v60, v60\n\t"
      "v_exp_f32 v61, v61\n\t"
      "v_mfma_f32_32x32x16_bf16 %[F01], v[124:127], v[112:115], %[F01]\n\t"
      "buffer_load_dwordx4 v[112:115], %[oh0], s[60:63], %[so] offen offset:1024\n\t"
      "v_exp_f32 v62, v62\n\t"
      "v_exp_f32 v63, v63\n\t"
      "v_exp_f32 v32, v32\n\t"
      "v_exp_f32 v33, v33\n\t"
      "v_exp_f32 v34, v34\n\t"
      "s_waitcnt lgkmcnt(2)\n\t"
      "v_smfmac_f32_32x32x32_bf16 %[O], v[88:91], v[56:63], %[idx]\n\t"
      "ds_read_b128 v[88:91], v130 offset:12384\n\t"
      "v_exp_f32 v35, v35\n\t"
      "v_exp_f32 v36, v36\n\t"
      "v_exp_f32 v37, v37\n\t"
      "v_exp_f32 v38, v38\n\t"
      "s_waitcnt lgkmcnt(2)\n\t"
      "v_mfma_f32_32x32x16_bf16 v[48:63], v[80:83], v[92:95], v[64:79]\n\t"
      "ds_read_b128 v[80:83], v128 offset:16512\n\t"
      "v_exp_f32 v39, v39\n\t"
      "v_exp_f32 v40, v40\n\t"
      "v_exp_f32 v41, v41\n\t"
      "v_exp_f32 v42, v42\n\t"
      "s_waitcnt lgkmcnt(2)\n\t"
      "v_smfmac_f32_32x32x32_bf16 %[O], v[84:87], v[32:39], %[idx]\n\t"
      "ds_read_b128 v[84:87], v129 offset:14448\n\t"
      "v_exp_f32 v43, v43\n\t"
      "v_exp_f32 v44, v44\n\t"
      "v_exp_f32 v45, v45\n\t"
      "s_waitcnt vmcnt(5)\n\t"
      "v_mfma_f32_32x32x16_bf16 %[F11], v[124:127], v[116:119], %[F11]\n\t"
      "buffer_load_dwordx4 v[124:127], %[ow1], s[60:63], %[so] offen offset:1024\n\t"
      "s_sub_u32 %[cnt], %[cnt], 1\n\t"
      "v_exp_f32 v46, v46\n\t"
      "v_exp_f32 v47, v47\n\t"
      "v_exp_f32 v48, v48\n\t"
      "v_exp_f32 v49, v49\n\t"
      "v_exp_f32 v50, v50\n\t"
      "s_waitcnt lgkmcnt(2)\n\t"
      "v_smfmac_f32_32x32x32_bf16 %[O], v[88:91], v[40:47], %[idx]\n\t"
      "ds_read_b128 v[88:91], v130 offset:14448\n\t"
      "v_exp_f32 v51, v51\n\t"
      "v_exp_f32 v52, v52\n\t"
      "v_exp_f32 v53, v53\n\t"
      "v_exp_f32 v54, v54\n\t"
      "s_waitcnt lgkmcnt(2)\n\t"
      "v_mfma_f32_32x32x16_bf16 v[32:47], v[80:83], v[92:95], v[64:79]\n\t"
      "ds_read_b128 v[80:83], v128 offset:18576\n\t"
      "v_exp_f32 v55, v55\n\t"
      "v_exp_f32 v56, v56\n\t"
      "v_exp_f32 v57, v57\n\t"
      "v_exp_f32 v58, v58\n\t"
      "s_waitcnt lgkmcnt(2)\n\t"
      "v_smfmac_f32_32x32x32_bf16 %[O], v[84:87], v[48:55], %[idx]\n\t"
      "ds_read_b128 v[84:87], v129 offset:16512\n\t"
      "v_exp_f32 v59, v59\n\t"
      "v_exp_f32 v60, v60\n\t"
      "v_exp_f32 v61, v61\n\t"
      "v_mfma_f32_32x32x16_bf16 %[F10], v[120:123], v[116:119], %[F10]\n\t"
      "buffer_load_dwordx4 v[120:123], %[ow0], s[60:63], %[so] offen offset:1024\n\t"
      "buffer_load_dwordx4 v[116:119], %[oh1], s[60:63], %[so] offen offset:1024\n\t"
      "v_add_u32 v128, 16512, v128\n\t"
      "v_add_u32 v129, 16512, v129\n\t"
      "v_add_u32 v130, 16512, v130\n\t"
      "s_cmp_lg_u32 %[cnt], 0\n\t"
      "v_exp_f32 v62, v62\n\t"
      "v_exp_f32 v63, v63\n\t"
      "s_cbranch_scc1 3b\n\t"
      "s_waitcnt lgkmcnt(2)\n\t"
      "v_smfmac_f32_32x32x32_bf16 %[O], v[88:91], v[56:63], %[idx]\n\t"
      "s_waitcnt vmcnt(0) lgkmcnt(0)\n\t"
      "s_nop 15\n\t"
      "s_nop 3\n\t"
      : [O] "=&v"(O), [F00] "=&v"(F00), [F01] "=&v"(F01), [F10] "=&v"(F10), [F11] "=&v"(F11), [so] "=&s"(so), [cnt] "=&s"(cnt), [ph] "=&s"(ph)
      : [ka] "v"(ka), [va0] "v"(va0), [va1] "v"(va1), [idx] "v"(idx), [qa] "v"(lds0 + QSLOT + w * 1024 + l * 16),
        [oh0] "v"(oh0), [oh1] "v"(oh0 + 112 * 1024), [ow0] "v"(ow0), [ow1] "v"(ow0 + 144 * 1024), [ws] "s"(ws), [wsz] "i"((unsigned)WS_END)
      : "v32", "v33", "v34", "v35", "v36", "v37", "v38", "v39", "v40", "v41", "v42", "v43", "v44", "v45", "v46", "v47", "v48", "v49", "v50", "v51", "v52", "v53", "v54", "v55", "v56", "v57", "v58", "v59", "v60", "v61", "v62", "v63", "v64", "v65", "v66", "v67", "v68", "v69", "v70", "v71", "v72", "v73", "v74", "v75", "v76", "v77", "v78", "v79", "v80", "v81", "v82", "v83", "v84", "v85", "v86", "v87", "v88", "v89", "v90", "v91", "v92", "v93", "v94", "v95", "v96", "v97", "v98", "v99", "v100", "v101", "v102", "v103", "v104", "v105", "v106", "v107", "v108", "v109", "v110", "v111", "v112", "v113", "v114", "v115", "v116", "v117", "v118", "v119", "v120", "v121", "v122", "v123", "v124", "v125", "v126", "v127", "v128", "v129", "v130", "v131", "v132", "s60", "s61", "s62", "s63", "scc", "memory");
  }
  STAMP(2);
  float lsum = h ? O[12] : O[8];
  {
    float mo = fabsf(O[0]);
#pragma unroll
    for (int r = 1; r < 8; ++r) mo = fmaxf(mo, fabsf(O[r]));
    const bool ok = (lsum <= 1.8e19f) && (mo <= 3.0e38f);
    if (__builtin_expect(!__all(ok), 0)) {
      const u32x4 qf = lds_r128(lds + QSLOT + w * 1024 + l * 16);
      float mrun = -3.0e38f; O = f32x16{};
#pragma unroll 1
      for (int tt = 0; tt < 64; ++tt) {
        f32x16 zero = {};
        f32x16 S = MFMA32(as_bf(lds_r128(lds + (ka - lds0) + tt * KVSTRIDE)), as_bf(qf), zero);
        const float mn = fmaxf(mrun, max16(S));
        const float al = __builtin_amdgcn_exp2f(mrun - mn); mrun = mn;
#pragma unroll
        for (int r = 0; r < 16; ++r) { O[r] *= al; S[r] = __builtin_amdgcn_exp2f(S[r] - mn); }
        O = smf(lds_r128(lds + (va0 - lds0) + tt * KVSTRIDE), lo8(S), O, idx);
        O = smf(lds_r128(lds + (va1 - lds0) + tt * KVSTRIDE), hi8(S), O, idx);
      }
      lsum = h ? O[12] : O[8];
    }
  }
  {
    const float inv = 1.0f / lsum;
    *(u32x4*)(ws + WS_O + (size_t)(qt * 32 + head) * 1024 + l * 16) =
        u32x4{pk2(O[0] * inv, O[1] * inv), pk2(O[2] * inv, O[3] * inv), pk2(O[4] * inv, O[5] * inv), pk2(O[6] * inv, O[7] * inv)};
  }
  STAMP(3);
  asm volatile("s_waitcnt lgkmcnt(0)\n\ts_barrier" ::: "memory");
  {
    ldsp dst = lds + w * 16384 + l * 16;
#pragma unroll
    for (int g = 0; g < 4; ++g) {
      *(LAS f32x4*)(dst + (0 * 4 + g) * 1024) = f32x4{F00[4 * g], F00[4 * g + 1], F00[4 * g + 2], F00[4 * g + 3]};
      *(LAS f32x4*)(dst + (1 * 4 + g) * 1024) = f32x4{F01[4 * g], F01[4 * g + 1], F01[4 * g + 2], F01[4 * g + 3]};
      *(LAS f32x4*)(dst + (2 * 4 + g) * 1024) = f32x4{F10[4 * g], F10[4 * g + 1], F10[4 * g + 2], F10[4 * g + 3]};
      *(LAS f32x4*)(dst + (3 * 4 + g) * 1024) = f32x4{F11[4 * g], F11[4 * g + 1], F11[4 * g + 2], F11[4 * g + 3]};
    }
  }
  const int ncol = nb * 64 + ((w >> 1) & 1) * 32 + (w & 1) * 16 + 4 * h;
  const u32x2 bb0 = *(const u32x2*)(b2 + ncol * 2), bb1 = *(const u32x2*)(b2 + (ncol + 8) * 2);
  asm volatile("s_waitcnt lgkmcnt(0)\n\ts_barrier" ::: "memory");
  {
    ldsp src = lds + w * 2048 + l * 16;
    f32x4 a = {bf_lo(bb0[0]), bf_hi(bb0[0]), bf_lo(bb0[1]), bf_hi(bb0[1])}, b = {bf_lo(bb1[0]), bf_hi(bb1[0]), bf_lo(bb1[1]), bf_hi(bb1[1])};
#pragma unroll
    for (int s = 0; s < 8; ++s) { a += *(LAS f32x4*)(src + s * 16384); b += *(LAS f32x4*)(src + s * 16384 + 1024); }
#pragma unroll
    for (int e = 0; e < 4; ++e) {
      gout[e] = a[e] / (1.0f + __expf(-a[e]));
      gout[4 + e] = b[e] / (1.0f + __expf(-b[e]));
    }
  }
  STAMP(4);
  STAMP_FLUSH(1, 10);
}

DI void phase5(const char* x, char* ws, char* out, ldsp lds, const float (&g)[8]) {
  const int tid = threadIdx.x, l = tid & 63;
  const int w = __builtin_amdgcn_readfirstlane(tid >> 6);
  const int xcd = blockIdx.x & 7, jj = blockIdx.x >> 3;
  const int tb = xcd * 4 + (jj >> 3), nb = jj & 7;
  const int t = l & 31, h = l >> 5;
  const int tt = w >> 2, nt = (w >> 1) & 1, kh = w & 1;
  const char* osrc = ws + WS_O + (size_t)((2 * tb + tt) * 32 + 16 * kh) * 1024 + l * 16;
  const char* wsrc = ws + WS_W + (size_t)((2 * nb + nt) * 144 + 112 + 16 * kh) * 1024 + l * 16;
  STAMP_DECL(10); STAMP(0);
  u32x4 wf[16], of[16];
#pragma unroll
  for (int s = 0; s < 16; ++s) wf[s] = *(const u32x4*)(wsrc + s * 1024);
#pragma unroll
  for (int s = 0; s < 16; ++s) of[s] = *(const u32x4*)(osrc + s * 1024);
  const int token = tb * 64 + tt * 32 + t;
  const int ncol = nb * 64 + nt * 32 + kh * 16 + 4 * h;
  const u32x2 x0 = *(const u32x2*)(x + (size_t)token * 1024 + ncol * 2), x1 = *(const u32x2*)(x + (size_t)token * 1024 + (ncol + 8) * 2);
  f32x16 S = {};
#pragma unroll
  for (int s = 0; s < 16; ++s) S = MFMA32(as_bf(wf[s]), as_bf(of[s]), S);
  PIN(S); STAMP(1);
  {
    ldsp dst = lds + 131072 + w * 2048 + l * 16;
    f32x4 a = kh ? f32x4{S[0], S[1], S[2], S[3]} : f32x4{S[8], S[9], S[10], S[11]};
    f32x4 b = kh ? f32x4{S[4], S[5], S[6], S[7]} : f32x4{S[12], S[13], S[14], S[15]};
    *(LAS f32x4*)dst = a; *(LAS f32x4*)(dst + 1024) = b;
  }
  asm volatile("s_waitcnt lgkmcnt(0)\n\ts_barrier" ::: "memory");
  ldsp src = lds + 131072 + (w ^ 1) * 2048 + l * 16;
  f32x4 a = *(LAS f32x4*)src, b = *(LAS f32x4*)(src + 1024);
  a += kh ? f32x4{S[8], S[9], S[10], S[11]} : f32x4{S[0], S[1], S[2], S[3]};
  b += kh ? f32x4{S[12], S[13], S[14], S[15]} : f32x4{S[4], S[5], S[6], S[7]};
  u32x2 r0 = {pk2(fmaf(a[0], g[0], bf_lo(x0[0])), fmaf(a[1], g[1], bf_hi(x0[0]))), pk2(fmaf(a[2], g[2], bf_lo(x0[1])), fmaf(a[3], g[3], bf_hi(x0[1])))};
  u32x2 r1 = {pk2(fmaf(b[0], g[4], bf_lo(x1[0])), fmaf(b[1], g[5], bf_hi(x1[0]))), pk2(fmaf(b[2], g[6], bf_lo(x1[1])), fmaf(b[3], g[7], bf_hi(x1[1])))};
  *(u32x2*)(out + (size_t)token * 1024 + ncol * 2) = r0;
  *(u32x2*)(out + (size_t)token * 1024 + (ncol + 8) * 2) = r1;
  STAMP(2);
  STAMP_FLUSH(2, 10);
}

extern __shared__ __attribute__((aligned(16))) char smem[];

__global__ __launch_bounds__(512, 2) void k1(const char* x, const char* wqkv, const char* wo, const char* w1, const char* b1, const char* w2, char* ws) {
  phase12(x, wqkv, wo, w1, b1, w2, ws, (ldsp)smem);
}
__global__ __launch_bounds__(512, 2) void k2(const char* b2, char* ws) {
  float g[8];
  phase3(b2, ws, (ldsp)smem, g);
  f32x4* gp = (f32x4*)(ws + WS_G + ((size_t)blockIdx.x * 512 + threadIdx.x) * 32);
  gp[0] = f32x4{g[0], g[1], g[2], g[3]}; gp[1] = f32x4{g[4], g[5], g[6], g[7]};
}
__global__ __launch_bounds__(512, 2) void k3(const char* x, char* ws, char* out) {
  const f32x4* gp = (const f32x4*)(ws + WS_G + ((size_t)blockIdx.x * 512 + threadIdx.x) * 32);
  f32x4 a = gp[0], b = gp[1];
  float g[8] = {a[0], a[1], a[2], a[3], b[0], b[1], b[2], b[3]};
  phase5(x, ws, out, (ldsp)smem, g);
}

extern "C" void kernel_launch(void* const* d_in, const int* in_sizes, int n_in, void* d_out, int out_size, void* d_ws, size_t ws_size, hipStream_t stream) {
  const char* x = (const char*)d_in[0]; const char* wqkv = (const char*)d_in[1]; const char* wo = (const char*)d_in[2];
  const char* w1 = (const char*)d_in[4]; const char* b1 = (const char*)d_in[5]; const char* w2 = (const char*)d_in[6]; const char* b2 = (const char*)d_in[7];
  (void)hipFuncSetAttribute((const void*)k1, hipFuncAttributeMaxDynamicSharedMemorySize, LDS_BYTES);
  (void)hipFuncSetAttribute((const void*)k2, hipFuncAttributeMaxDynamicSharedMemorySize, LDS_BYTES);
  (void)hipFuncSetAttribute((const void*)k3, hipFuncAttributeMaxDynamicSharedMemorySize, LDS_BYTES);
  k1<<<256, 512, LDS_BYTES, stream>>>(x, wqkv, wo, w1, b1, w2, (char*)d_ws);
  k2<<<256, 512, LDS_BYTES, stream>>>(b2, (char*)d_ws);
  k3<<<256, 512, LDS_BYTES, stream>>>(x, (char*)d_ws, (char*)d_out);
}
```

```cpp
#include <hip/hip_runtime.h>
#include <hip/hip_bf16.h>
#include <stdint.h>

typedef __attribute__((ext_vector_type(8))) __bf16 bf16x8;
typedef __attribute__((ext_vector_type(16))) __bf16 bf16x16;
typedef __attribute__((ext_vector_type(2))) __bf16 bf16x2;
typedef __attribute__((ext_vector_type(4))) short s16x4;
typedef __attribute__((ext_vector_type(32))) float f32x32;
typedef __attribute__((ext_vector_type(16))) float f32x16;
typedef __attribute__((ext_vector_type(8))) float f32x8;
typedef __attribute__((ext_vector_type(4))) float f32x4;
typedef __attribute__((ext_vector_type(2))) float f32x2;
typedef __attribute__((ext_vector_type(4))) unsigned u32x4;
typedef __attribute__((ext_vector_type(2))) unsigned u32x2;
typedef unsigned long long u64;
#define LAS __attribute__((address_space(3)))
#define GAS __attribute__((address_space(1)))
#define DI __device__ __forceinline__
typedef LAS char* ldsp;

constexpr unsigned WS_EPOCH = 0;
constexpr unsigned WS_FQ = 1024, WS_FH = WS_FQ + 16384, WS_FO = WS_FH + 16384;
constexpr unsigned WS_Q = 65536;
constexpr unsigned WS_KV = WS_Q + 2097152;
constexpr unsigned WS_H = WS_KV + 4194304;
constexpr unsigned WS_O = WS_H + 64 * 112 * 1024;
constexpr unsigned WS_W = WS_O + 2097152;
constexpr unsigned WS_END = WS_W + 16 * 144 * 1024;
constexpr int LDS_BYTES = 163840;
constexpr int VSCR = 131072, QSLOT = 139264, MISC = 147456, KVSTRIDE = 2080;
constexpr int EXL = 135424, CNT = 137728;

#define MFMA32(a, b, c) __builtin_amdgcn_mfma_f32_32x32x16_bf16((a), (b), (c), 0, 0, 0)
#define PIN(x) asm volatile("" : "+v"(x))
#define WAIT_BAR(N) asm volatile("s_waitcnt vmcnt(%0) lgkmcnt(0)\n\ts_barrier" ::"i"(N) : "memory")

#define STAMP(i)
#define STAMP_DECL()
#define STAMP_FLUSH()
#define EXB MISC
#define SPART 131072

DI bf16x8 as_bf(u32x4 v) { return __builtin_bit_cast(bf16x8, v); }
DI unsigned pk2(float lo, float hi) { bf16x2 r = {(__bf16)lo, (__bf16)hi}; return __builtin_bit_cast(unsigned, r); }
DI float bf_lo(unsigned u) { return __uint_as_float(u << 16); }
DI float bf_hi(unsigned u) { return __uint_as_float(u & 0xffff0000u); }
DI float swap_add(float v) { auto r = __builtin_amdgcn_permlane32_swap(__float_as_uint(v), __float_as_uint(v), false, false); return __uint_as_float(r[0]) + __uint_as_float(r[1]); }
DI float swap_max(float v) { auto r = __builtin_amdgcn_permlane32_swap(__float_as_uint(v), __float_as_uint(v), false, false); return fmaxf(__uint_as_float(r[0]), __uint_as_float(r[1])); }

DI void glds2(const char* sbase, unsigned v0, unsigned v1, unsigned dst) {
  unsigned keep;
  asm volatile("s_mov_b32 %0, m0\n\ts_mov_b32 m0, %4\n\ts_nop 0\n\tglobal_load_lds_dwordx4 %1, %3\n\tglobal_load_lds_dwordx4 %2, %3 offset:1024\n\ts_mov_b32 m0, %0"
               : "=&s"(keep) : "v"(v0), "v"(v1), "s"(sbase), "s"(dst) : "memory");
}
DI void glds2_dev(const char* sbase, unsigned v0, unsigned dst) {
  unsigned keep;
  asm volatile("s_mov_b32 %0, m0\n\ts_mov_b32 m0, %3\n\ts_nop 0\n\tglobal_load_lds_dwordx4 %1, %2 sc1\n\tglobal_load_lds_dwordx4 %1, %2 offset:1024 sc1\n\ts_mov_b32 m0, %0"
               : "=&s"(keep) : "v"(v0), "s"(sbase), "s"(dst) : "memory");
}
DI void glds1_dev(const char* sbase, unsigned v0, unsigned dst) {
  unsigned keep;
  asm volatile("s_mov_b32 %0, m0\n\ts_mov_b32 m0, %3\n\ts_nop 0\n\tglobal_load_lds_dwordx4 %1, %2 sc1\n\ts_mov_b32 m0, %0"
               : "=&s"(keep) : "v"(v0), "s"(sbase), "s"(dst) : "memory");
}
DI u32x4 lds_r128(ldsp p) { return *(LAS u32x4*)p; }
DI u32x4 lds_tr2(ldsp p0, ldsp p1) {
  s16x4 a = __builtin_amdgcn_ds_read_tr16_b64_v4i16((LAS s16x4*)p0);
  s16x4 b = __builtin_amdgcn_ds_read_tr16_b64_v4i16((LAS s16x4*)p1);
  u32x2 ua = __builtin_bit_cast(u32x2, a), ub = __builtin_bit_cast(u32x2, b);
  return u32x4{ua[0], ua[1], ub[0], ub[1]};
}
#define ST_DEV(v, off) __builtin_amdgcn_raw_buffer_store_b128((v), rsrc, (off), 0, 16)
#define LD_DEV(off) __builtin_amdgcn_raw_buffer_load_b128(rsrc, (off), 0, 16)
DI u64 ld_flag(char* ws, unsigned off) { return __hip_atomic_load((GAS u64*)(ws + off), __ATOMIC_RELAXED, __HIP_MEMORY_SCOPE_AGENT); }
DI void st_flag(char* ws, unsigned off, u64 v) { __hip_atomic_store((GAS u64*)(ws + off), v, __ATOMIC_RELAXED, __HIP_MEMORY_SCOPE_AGENT); }
DI void poll_flags(char* ws, unsigned off, u64 tag, u64 first) {
  if (__all(first == tag)) return;
#pragma unroll 1
  for (unsigned spin = 0; spin < (1u << 18); ++spin) {
    __builtin_amdgcn_s_sleep(2);
    if (__all(ld_flag(ws, off) == tag)) break;
  }
}
DI f32x16 smf(u32x4 a, f32x8 p, f32x16 c, int idx) {
  return __builtin_amdgcn_smfmac_f32_32x32x32_bf16(as_bf(a), __builtin_bit_cast(bf16x16, p), c, idx, 0, 0);
}
DI f32x8 lo8(const f32x16& v) { return __builtin_shufflevector(v, v, 0, 1, 2, 3, 4, 5, 6, 7); }
DI f32x8 hi8(const f32x16& v) { return __builtin_shufflevector(v, v, 8, 9, 10, 11, 12, 13, 14, 15); }
DI float max16(const f32x16& s) {
  float a = fmaxf(fmaxf(s[0], s[1]), s[2]), b = fmaxf(fmaxf(s[3], s[4]), s[5]);
  a = fmaxf(fmaxf(a, s[6]), s[7]); b = fmaxf(fmaxf(b, s[8]), s[9]); a = fmaxf(fmaxf(a, s[10]), s[11]); b = fmaxf(fmaxf(b, s[12]), s[13]);
  a = fmaxf(fmaxf(a, s[14]), s[15]);
  return swap_max(fmaxf(a, b));
}

extern __shared__ __attribute__((aligned(16))) char smem[];

__global__ __launch_bounds__(512, 2) void fused_block(const char* x, const char* wqkv, const char* wo, const char* w1, const char* b1,
                                                      const char* w2, const char* b2, char* ws, char* out) {
  const ldsp lds = (ldsp)smem;
  int tid = threadIdx.x, l = tid & 63;
  const int w = __builtin_amdgcn_readfirstlane(tid >> 6);
  int t = l & 31, h = l >> 5, q4 = (l >> 2) & 3;
#define RELANE() do { PIN(tid); l = tid & 63; t = l & 31; h = l >> 5; q4 = (l >> 2) & 3; } while (0)
  const int xcd = blockIdx.x & 7, jj = blockIdx.x >> 3;
  STAMP_DECL(); STAMP(0);
  const int m = w & 3, g2 = w >> 2;
#define CB_OF(xc, j) (((xc) & 1) * 8 + ((j) & 7))
#define RB_OF(xc, j) (((xc) >> 1) * 4 + ((j) >> 3))
  const int cb = CB_OF(xcd, jj), rb = RB_OF(xcd, jj);
#define CSWZ ((unsigned)(((l & 15) ^ ((l >> 4) << 2)) << 4))
  const unsigned vx0 = (32 * m + (l >> 3)) * 1024 + (((l & 7) ^ (l >> 4)) << 4);
  const unsigned vx1 = (32 * m + 8 + (l >> 3)) * 1024 + (((l & 7) ^ (4 + (l >> 4))) << 4) - 1024;
  const char* xs = x + (size_t)rb * 128 * 1024;
  asm volatile(
      "s_mov_b32 s68, m0\n\t"
      "s_add_u32 m0, %[d0], 0\n\t"
      "s_nop 0\n\t"
      "global_load_lds_dwordx4 %[vx0], %[xs0]\n\t"
      "global_load_lds_dwordx4 %[vx1], %[xs0] offset:1024\n\t"
      "s_add_u32 m0, %[d0], 65536\n\t"
      "s_nop 0\n\t"
      "global_load_lds_dwordx4 %[vx0], %[xs1]\n\t"
      "global_load_lds_dwordx4 %[vx1], %[xs1] offset:1024\n\t"
      "s_mov_b32 m0, s68\n\t"
      :: [vx0] "v"(vx0), [vx1] "v"(vx1), [xs0] "s"(xs + g2 * 16384), [xs1] "s"(xs + g2 * 16384 + 128), [d0] "s"(m * 4096 + g2 * 2048)
      : "s68", "scc", "memory");
  __builtin_amdgcn_sched_barrier(0);
  const __amdgpu_buffer_rsrc_t rsrc = __builtin_amdgcn_make_buffer_rsrc(ws, 0, WS_END, 0x00020000);
  const int head = xcd * 4 + (jj >> 3), qb = jj & 7;
  const int tb = xcd * 4 + (jj >> 3), nb = jj & 7;
  const int qp = w & 3, kh = w >> 2;
  const bool tr_role = cb >= 14;
  const int z = rb * 2 + (cb - 14);
  const unsigned st2 = tr_role ? 1024 : 3584;
#define VW0(ww) ((8 * (ww) + (l >> 4)) * st2 + CSWZ)
#define VW1(ww) (VW0(ww) + 4 * st2 - 1024)
  auto p2src = [&](int s) -> const char* {
    int kc = (z >> 2) + 8 * s; kc = kc < 36 ? kc : 0;
    const char* pt = kc < 28 ? w2 : wo - 28 * 65536;
    const int offn = s * 64 * 3584 + cb * 256, offt = kc * 65536 + (z & 3) * 256;
    return (tr_role ? pt : w1) + (tr_role ? offt : offn);
  };
  if (g2) {
    unsigned keep;
    const char* src = (!(w & 1) || tr_role) ? (const char*)ws : b1 + cb * 256;
    asm volatile("s_mov_b32 %0, m0\n\ts_mov_b32 m0, %3\n\ts_nop 0\n\tglobal_load_lds_dword %1, %2 sc1\n\ts_mov_b32 m0, %0"
                 : "=&s"(keep) : "v"(l * 4), "s"(src), "s"(MISC + 10496 - (w & 1) * 256) : "memory");
  }
#define WLANE ((unsigned)(h * 2048 + q4 * 256 + ((l >> 4) & 1) * 32 + (l & 3) * 8 + (m << 12) + 65536))
#define CSEL(half) (((l & 2) == 0 && ((l >> 3) & 1) == h && ((l >> 2) & 1) == (half)) ? 0x3f803f80u : 0u)
#define CIDX ((l & 1) ? 0xEEEEu : 0x4444u)
  f32x32 xf0, xf1, xf2, xf3, acc; f32x4 st;
  {
    const unsigned vq0 = (16 * m + (l >> 4)) * 3072 + CSWZ, vq1 = vq0 + 4 * 3072 - 1024;
    const char* qs = wqkv + ((96 * cb) & ~63) * 2;
    const unsigned xb = ((32 * m + t) * 128 + ((h ^ ((t >> 1) & 7)) << 4)) ^ (m << 5);
    const int T0 = (cb & 1) + 2 * g2;
    const unsigned wb0 = WLANE + ((T0 ^ q4) << 6), wb1 = WLANE + ((((T0 + 1) & 3) ^ q4) << 6);
    asm volatile("s_cmp_ge_u32 %[wv], 4\n\ts_cbranch_scc1 20f\n\t"
      "s_mov_b32 s68, m0\n\t"
      "s_mov_b64 s[70:71], %[xs]\n\t"
      "s_mov_b64 s[72:73], %[qs]\n\t"
      "s_mov_b32 s69, 0x3f803f80\n\t"
      "s_add_u32 s64, s70, 256\n\t"
      "s_addc_u32 s65, s71, 0\n\t"
      "s_add_u32 m0, %[dst4], 16384\n\t"
      "s_nop 0\n\t"
      "global_load_lds_dwordx4 %[vx0], s[64:65]\n\t"
      "global_load_lds_dwordx4 %[vx1], s[64:65] offset:1024\n\t"
      "s_add_u32 s64, s64, 16384\n\t"
      "s_addc_u32 s65, s65, 0\n\t"
      "s_add_u32 m0, %[dst4], 18432\n\t"
      "s_nop 0\n\t"
      "global_load_lds_dwordx4 %[vx0], s[64:65]\n\t"
      "global_load_lds_dwordx4 %[vx1], s[64:65] offset:1024\n\t"
      "s_add_u32 s64, s70, 384\n\t"
      "s_addc_u32 s65, s71, 0\n\t"
      "s_add_u32 m0, %[dst4], 81920\n\t"
      "s_nop 0\n\t"
      "global_load_lds_dwordx4 %[vx0], s[64:65]\n\t"
      "global_load_lds_dwordx4 %[vx1], s[64:65] offset:1024\n\t"
      "s_add_u32 s64, s64, 16384\n\t"
      "s_addc_u32 s65, s65, 0\n\t"
      "s_add_u32 m0, %[dst4], 83968\n\t"
      "s_nop 0\n\t"
      "global_load_lds_dwordx4 %[vx0], s[64:65]\n\t"
      "global_load_lds_dwordx4 %[vx1], s[64:65] offset:1024\n\t"
      "v_xor_b32 v48, 0, %[xb]\n\t"
      "v_add_u32 v28, 0x10000, v48\n\t"
      "v_xor_b32 v52, 0, %[wb0]\n\t"
      "v_xor_b32 v56, 0, %[wb1]\n\t"
      "v_xor_b32 v49, 32, %[xb]\n\t"
      "v_add_u32 v29, 0x10000, v49\n\t"
      "v_xor_b32 v53, 4096, %[wb0]\n\t"
      "v_xor_b32 v57, 4096, %[wb1]\n\t"
      "v_xor_b32 v50, 64, %[xb]\n\t"
      "v_add_u32 v30, 0x10000, v50\n\t"
      "v_xor_b32 v54, 8192, %[wb0]\n\t"
      "v_xor_b32 v58, 8192, %[wb1]\n\t"
      "v_xor_b32 v51, 96, %[xb]\n\t"
      "v_add_u32 v31, 0x10000, v51\n\t"
      "v_xor_b32 v55, 12288, %[wb0]\n\t"
      "v_xor_b32 v59, 12288, %[wb1]\n\t"
      "v_subrev_u32 v32, 0x10000, v52\n\t"
      "v_subrev_u32 v36, 0x10000, v56\n\t"
      "v_subrev_u32 v33, 0x10000, v53\n\t"
      "v_subrev_u32 v37, 0x10000, v57\n\t"
      "v_subrev_u32 v34, 0x10000, v54\n\t"
      "v_subrev_u32 v38, 0x10000, v58\n\t"
      "v_subrev_u32 v35, 0x10000, v55\n\t"
      "v_subrev_u32 v39, 0x10000, v59\n\t"
      "v_mov_b32 v40, %[calo]\n\t"
      "v_mov_b32 v41, %[calo]\n\t"
      "v_mov_b32 v42, %[cahi]\n\t"
      "v_mov_b32 v43, %[cahi]\n\t"
      "v_mov_b32 v44, %[cidx]\n\t"
      "s_add_u32 s64, s70, 512\n\t"
      "s_addc_u32 s65, s71, 0\n\t"
      "s_add_u32 m0, %[dst4], 32768\n\t"
      "s_nop 0\n\t"
      "global_load_lds_dwordx4 %[vx0], s[64:65]\n\t"
      "global_load_lds_dwordx4 %[vx1], s[64:65] offset:1024\n\t"
      "s_add_u32 s64, s64, 16384\n\t"
      "s_addc_u32 s65, s65, 0\n\t"
      "s_add_u32 m0, %[dst4], 34816\n\t"
      "s_nop 0\n\t"
      "global_load_lds_dwordx4 %[vx0], s[64:65]\n\t"
      "global_load_lds_dwordx4 %[vx1], s[64:65] offset:1024\n\t"
      "s_add_u32 s64, s70, 640\n\t"
      "s_addc_u32 s65, s71, 0\n\t"
      "s_add_u32 m0, %[dst4], 98304\n\t"
      "s_nop 0\n\t"
      "global_load_lds_dwordx4 %[vx0], s[64:65]\n\t"
      "global_load_lds_dwordx4 %[vx1], s[64:65] offset:1024\n\t"
      "s_add_u32 s64, s64, 16384\n\t"
      "s_addc_u32 s65, s65, 0\n\t"
      "s_add_u32 m0, %[dst4], 100352\n\t"
      "s_nop 0\n\t"
      "global_load_lds_dwordx4 %[vx0], s[64:65]\n\t"
      "global_load_lds_dwordx4 %[vx1], s[64:65] offset:1024\n\t"
      "s_waitcnt vmcnt(16) lgkmcnt(0)\n\t"
      "s_barrier\n\t"
      "ds_read_b128 v[128:131], v48 offset:0\n\t"
      "ds_read_b128 v[132:135], v49 offset:0\n\t"
      "ds_read_b128 v[136:139], v50 offset:0\n\t"
      "ds_read_b128 v[140:143], v51 offset:0\n\t"
      "ds_read_b128 v[144:147], v28 offset:0\n\t"
      "ds_read_b128 v[148:151], v29 offset:0\n\t"
      "ds_read_b128 v[152:155], v30 offset:0\n\t"
      "ds_read_b128 v[156:159], v31 offset:0\n\t"
      "s_add_u32 s64, s70, 768\n\t"
      "s_addc_u32 s65, s71, 0\n\t"
      "s_add_u32 m0, %[dst4], 49152\n\t"
      "s_nop 0\n\t"
      "global_load_lds_dwordx4 %[vx0], s[64:65]\n\t"
      "global_load_lds_dwordx4 %[vx1], s[64:65] offset:1024\n\t"
      "s_add_u32 s64, s64, 16384\n\t"
      "s_addc_u32 s65, s65, 0\n\t"
      "s_add_u32 m0, %[dst4], 51200\n\t"
      "s_nop 0\n\t"
      "global_load_lds_dwordx4 %[vx0], s[64:65]\n\t"
      "global_load_lds_dwordx4 %[vx1], s[64:65] offset:1024\n\t"
      "s_add_u32 s64, s70, 896\n\t"
      "s_addc_u32 s65, s71, 0\n\t"
      "s_add_u32 m0, %[dst4], 114688\n\t"
      "s_nop 0\n\t"
      "global_load_lds_dwordx4 %[vx0], s[64:65]\n\t"
      "global_load_lds_dwordx4 %[vx1], s[64:65] offset:1024\n\t"
      "s_add_u32 s64, s64, 16384\n\t"
      "s_addc_u32 s65, s65, 0\n\t"
      "s_add_u32 m0, %[dst4], 116736\n\t"
      "s_nop 0\n\t"
      "global_load_lds_dwordx4 %[vx0], s[64:65]\n\t"
      "global_load_lds_dwordx4 %[vx1], s[64:65] offset:1024\n\t"
      "s_waitcnt vmcnt(16) lgkmcnt(0)\n\t"
      "s_barrier\n\t"
      "ds_read_b128 v[160:163], v48 offset:16384\n\t"
      "ds_read_b128 v[164:167], v49 offset:16384\n\t"
      "ds_read_b128 v[168:171], v50 offset:16384\n\t"
      "ds_read_b128 v[172:175], v51 offset:16384\n\t"
      "ds_read_b128 v[176:179], v28 offset:16384\n\t"
      "ds_read_b128 v[180:183], v29 offset:16384\n\t"
      "ds_read_b128 v[184:187], v30 offset:16384\n\t"
      "ds_read_b128 v[188:191], v31 offset:16384\n\t"
      "s_add_u32 s64, s72, 0\n\t"
      "s_addc_u32 s65, s73, 0\n\t"
      "s_add_u32 m0, %[dst4], 0\n\t"
      "s_nop 0\n\t"
      "global_load_lds_dwordx4 %[vq0], s[64:65]\n\t"
      "global_load_lds_dwordx4 %[vq1], s[64:65] offset:1024\n\t"
      "s_add_u32 s64, s64, 24576\n\t"
      "s_addc_u32 s65, s65, 0\n\t"
      "s_add_u32 m0, %[dst4], 2048\n\t"
      "s_nop 0\n\t"
      "global_load_lds_dwordx4 %[vq0], s[64:65]\n\t"
      "global_load_lds_dwordx4 %[vq1], s[64:65] offset:1024\n\t"
      "s_add_u32 s64, s72, 196608\n\t"
      "s_addc_u32 s65, s73, 0\n\t"
      "s_add_u32 m0, %[dst4], 65536\n\t"
      "s_nop 0\n\t"
      "global_load_lds_dwordx4 %[vq0], s[64:65]\n\t"
      "global_load_lds_dwordx4 %[vq1], s[64:65] offset:1024\n\t"
      "s_add_u32 s64, s64, 24576\n\t"
      "s_addc_u32 s65, s65, 0\n\t"
      "s_add_u32 m0, %[dst4], 67584\n\t"
      "s_nop 0\n\t"
      "global_load_lds_dwordx4 %[vq0], s[64:65]\n\t"
      "global_load_lds_dwordx4 %[vq1], s[64:65] offset:1024\n\t"
      "s_waitcnt vmcnt(16) lgkmcnt(0)\n\t"
      "s_barrier\n\t"
      "ds_read_b128 v[192:195], v48 offset:32768\n\t"
      "ds_read_b128 v[196:199], v49 offset:32768\n\t"
      "ds_read_b128 v[200:203], v50 offset:32768\n\t"
      "ds_read_b128 v[204:207], v51 offset:32768\n\t"
      "ds_read_b128 v[208:211], v28 offset:32768\n\t"
      "ds_read_b128 v[212:215], v29 offset:32768\n\t"
      "ds_read_b128 v[216:219], v30 offset:32768\n\t"
      "ds_read_b128 v[220:223], v31 offset:32768\n\t"
      "s_add_u32 s64, s72, 393216\n\t"
      "s_addc_u32 s65, s73, 0\n\t"
      "s_add_u32 m0, %[dst4], 16384\n\t"
      "s_nop 0\n\t"
      "global_load_lds_dwordx4 %[vq0], s[64:65]\n\t"
      "global_load_lds_dwordx4 %[vq1], s[64:65] offset:1024\n\t"
      "s_add_u32 s64, s64, 24576\n\t"
      "s_addc_u32 s65, s65, 0\n\t"
      "s_add_u32 m0, %[dst4], 18432\n\t"
      "s_nop 0\n\t"
      "global_load_lds_dwordx4 %[vq0], s[64:65]\n\t"
      "global_load_lds_dwordx4 %[vq1], s[64:65] offset:1024\n\t"
      "s_add_u32 s64, s72, 589824\n\t"
      "s_addc_u32 s65, s73, 0\n\t"
      "s_add_u32 m0, %[dst4], 81920\n\t"
      "s_nop 0\n\t"
      "global_load_lds_dwordx4 %[vq0], s[64:65]\n\t"
      "global_load_lds_dwordx4 %[vq1], s[64:65] offset:1024\n\t"
      "s_add_u32 s64, s64, 24576\n\t"
      "s_addc_u32 s65, s65, 0\n\t"
      "s_add_u32 m0, %[dst4], 83968\n\t"
      "s_nop 0\n\t"
      "global_load_lds_dwordx4 %[vq0], s[64:65]\n\t"
      "global_load_lds_dwordx4 %[vq1], s[64:65] offset:1024\n\t"
      "s_waitcnt vmcnt(16) lgkmcnt(0)\n\t"
      "s_barrier\n\t"
      "ds_read_b128 v[224:227], v48 offset:49152\n\t"
      "ds_read_b128 v[228:231], v49 offset:49152\n\t"
      "ds_read_b128 v[232:235], v50 offset:49152\n\t"
      "ds_read_b128 v[236:239], v51 offset:49152\n\t"
      "ds_read_b128 v[240:243], v28 offset:49152\n\t"
      "ds_read_b128 v[244:247], v29 offset:49152\n\t"
      "ds_read_b128 v[248:251], v30 offset:49152\n\t"
      "ds_read_b128 v[252:255], v31 offset:49152\n\t"
      "s_waitcnt vmcnt(8) lgkmcnt(0)\n\t"
      "s_barrier\n\t"
      "ds_read_b64_tr_b16 v[64:65], v32 offset:0\n\t"
      "ds_read_b64_tr_b16 v[66:67], v32 offset:1024\n\t"
      "ds_read_b64_tr_b16 v[68:69], v36 offset:0\n\t"
      "ds_read_b64_tr_b16 v[70:71], v36 offset:1024\n\t"
      "ds_read_b64_tr_b16 v[72:73], v33 offset:0\n\t"
      "ds_read_b64_tr_b16 v[74:75], v33 offset:1024\n\t"
      "ds_read_b64_tr_b16 v[76:77], v37 offset:0\n\t"
      "ds_read_b64_tr_b16 v[78:79], v37 offset:1024\n\t"
      "v_mov_b32 v48, 0\n\t"
      "v_mov_b32 v49, 0\n\t"
      "v_mov_b32 v50, 0\n\t"
      "v_mov_b32 v51, 0\n\t"
      "s_waitcnt lgkmcnt(0)\n\t"
      "v_mfma_f32_32x32x16_bf16 v[96:111], v[64:67], v[128:131], 0\n\t"
      "ds_read_b64_tr_b16 v[80:81], v34 offset:0\n\t"
      "ds_read_b64_tr_b16 v[82:83], v34 offset:1024\n\t"
      "ds_read_b64_tr_b16 v[84:85], v38 offset:0\n\t"
      "ds_read_b64_tr_b16 v[86:87], v38 offset:1024\n\t"
      "v_mfma_f32_32x32x16_bf16 v[112:127], v[68:71], v[128:131], 0\n\t"
      "ds_read_b64_tr_b16 v[88:89], v35 offset:0\n\t"
      "ds_read_b64_tr_b16 v[90:91], v35 offset:1024\n\t"
      "ds_read_b64_tr_b16 v[92:93], v39 offset:0\n\t"
      "ds_read_b64_tr_b16 v[94:95], v39 offset:1024\n\t"
      "v_mfma_f32_32x32x16_bf16 v[96:111], v[72:75], v[132:135], v[96:111]\n\t"
      "v_mfma_f32_32x32x16_bf16 v[112:127], v[76:79], v[132:135], v[112:127]\n\t"
      "v_smfmac_f32_16x16x64_bf16 v[48:51], v[40:43], v[64:71], v44\n\t"
      "s_waitcnt lgkmcnt(0)\n\t"
      "v_mfma_f32_32x32x16_bf16 v[96:111], v[80:83], v[136:139], v[96:111]\n\t"
      "ds_read_b64_tr_b16 v[64:65], v52 offset:0\n\t"
      "ds_read_b64_tr_b16 v[66:67], v52 offset:1024\n\t"
      "ds_read_b64_tr_b16 v[68:69], v56 offset:0\n\t"
      "ds_read_b64_tr_b16 v[70:71], v56 offset:1024\n\t"
      "v_mfma_f32_32x32x16_bf16 v[112:127], v[84:87], v[136:139], v[112:127]\n\t"
      "ds_read_b64_tr_b16 v[72:73], v53 offset:0\n\t"
      "ds_read_b64_tr_b16 v[74:75], v53 offset:1024\n\t"
      "ds_read_b64_tr_b16 v[76:77], v57 offset:0\n\t"
      "ds_read_b64_tr_b16 v[78:79], v57 offset:1024\n\t"
      "v_mfma_f32_32x32x16_bf16 v[96:111], v[88:91], v[140:143], v[96:111]\n\t"
      "v_mfma_f32_32x32x16_bf16 v[112:127], v[92:95], v[140:143], v[112:127]\n\t"
      "s_waitcnt lgkmcnt(0)\n\t"
      "v_mfma_f32_32x32x16_bf16 v[96:111], v[64:67], v[144:147], v[96:111]\n\t"
      "ds_read_b64_tr_b16 v[80:81], v54 offset:0\n\t"
      "ds_read_b64_tr_b16 v[82:83], v54 offset:1024\n\t"
      "ds_read_b64_tr_b16 v[84:85], v58 offset:0\n\t"
      "ds_read_b64_tr_b16 v[86:87], v58 offset:1024\n\t"
      "v_mfma_f32_32x32x16_bf16 v[112:127], v[68:71], v[144:147], v[112:127]\n\t"
      "ds_read_b64_tr_b16 v[88:89], v55 offset:0\n\t"
      "ds_read_b64_tr_b16 v[90:91], v55 offset:1024\n\t"
      "ds_read_b64_tr_b16 v[92:93], v59 offset:0\n\t"
      "ds_read_b64_tr_b16 v[94:95], v59 offset:1024\n\t"
      "v_mfma_f32_32x32x16_bf16 v[96:111], v[72:75], v[148:151], v[96:111]\n\t"
      "v_mfma_f32_32x32x16_bf16 v[112:127], v[76:79], v[148:151], v[112:127]\n\t"
      "v_smfmac_f32_16x16x64_bf16 v[48:51], v[40:43], v[64:71], v44\n\t"
      "s_waitcnt vmcnt(0) lgkmcnt(0)\n\t"
      "s_barrier\n\t"
      "v_mfma_f32_32x32x16_bf16 v[96:111], v[80:83], v[152:155], v[96:111]\n\t"
      "ds_read_b64_tr_b16 v[64:65], v32 offset:16384\n\t"
      "ds_read_b64_tr_b16 v[66:67], v32 offset:17408\n\t"
      "ds_read_b64_tr_b16 v[68:69], v36 offset:16384\n\t"
      "ds_read_b64_tr_b16 v[70:71], v36 offset:17408\n\t"
      "v_mfma_f32_32x32x16_bf16 v[112:127], v[84:87], v[152:155], v[112:127]\n\t"
      "ds_read_b64_tr_b16 v[72:73], v33 offset:16384\n\t"
      "ds_read_b64_tr_b16 v[74:75], v33 offset:17408\n\t"
      "ds_read_b64_tr_b16 v[76:77], v37 offset:16384\n\t"
      "ds_read_b64_tr_b16 v[78:79], v37 offset:17408\n\t"
      "v_mfma_f32_32x32x16_bf16 v[96:111], v[88:91], v[156:159], v[96:111]\n\t"
      "v_mfma_f32_32x32x16_bf16 v[112:127], v[92:95], v[156:159], v[112:127]\n\t"
      "s_waitcnt lgkmcnt(0)\n\t"
      "v_mfma_f32_32x32x16_bf16 v[96:111], v[64:67], v[160:163], v[96:111]\n\t"
      "ds_read_b64_tr_b16 v[80:81], v34 offset:16384\n\t"
      "ds_read_b64_tr_b16 v[82:83], v34 offset:17408\n\t"
      "ds_read_b64_tr_b16 v[84:85], v38 offset:16384\n\t"
      "ds_read_b64_tr_b16 v[86:87], v38 offset:17408\n\t"
      "v_mfma_f32_32x32x16_bf16 v[112:127], v[68:71], v[160:163], v[112:127]\n\t"
      "ds_read_b64_tr_b16 v[88:89], v35 offset:16384\n\t"
      "ds_read_b64_tr_b16 v[90:91], v35 offset:17408\n\t"
      "ds_read_b64_tr_b16 v[92:93], v39 offset:16384\n\t"
      "ds_read_b64_tr_b16 v[94:95], v39 offset:17408\n\t"
      "v_mfma_f32_32x32x16_bf16 v[96:111], v[72:75], v[164:167], v[96:111]\n\t"
      "v_mfma_f32_32x32x16_bf16 v[112:127], v[76:79], v[164:167], v[112:127]\n\t"
      "v_smfmac_f32_16x16x64_bf16 v[48:51], v[40:43], v[64:71], v44\n\t"
      "s_waitcnt lgkmcnt(0)\n\t"
      "v_mfma_f32_32x32x16_bf16 v[96:111], v[80:83], v[168:171], v[96:111]\n\t"
      "ds_read_b64_tr_b16 v[64:65], v52 offset:16384\n\t"
      "ds_read_b64_tr_b16 v[66:67], v52 offset:17408\n\t"
      "ds_read_b64_tr_b16 v[68:69], v56 offset:16384\n\t"
      "ds_read_b64_tr_b16 v[70:71], v56 offset:17408\n\t"
      "v_mfma_f32_32x32x16_bf16 v[112:127], v[84:87], v[168:171], v[112:127]\n\t"
      "ds_read_b64_tr_b16 v[72:73], v53 offset:16384\n\t"
      "ds_read_b64_tr_b16 v[74:75], v53 offset:17408\n\t"
      "ds_read_b64_tr_b16 v[76:77], v57 offset:16384\n\t"
      "ds_read_b64_tr_b16 v[78:79], v57 offset:17408\n\t"
      "v_mfma_f32_32x32x16_bf16 v[96:111], v[88:91], v[172:175], v[96:111]\n\t"
      "v_mfma_f32_32x32x16_bf16 v[112:127], v[92:95], v[172:175], v[112:127]\n\t"
      "s_waitcnt lgkmcnt(0)\n\t"
      "v_mfma_f32_32x32x16_bf16 v[96:111], v[64:67], v[176:179], v[96:111]\n\t"
      "ds_read_b64_tr_b16 v[80:81], v54 offset:16384\n\t"
      "ds_read_b64_tr_b16 v[82:83], v54 offset:17408\n\t"
      "ds_read_b64_tr_b16 v[84:85], v58 offset:16384\n\t"
      "ds_read_b64_tr_b16 v[86:87], v58 offset:17408\n\t"
      "v_mfma_f32_32x32x16_bf16 v[112:127], v[68:71], v[176:179], v[112:127]\n\t"
      "ds_read_b64_tr_b16 v[88:89], v55 offset:16384\n\t"
      "ds_read_b64_tr_b16 v[90:91], v55 offset:17408\n\t"
      "ds_read_b64_tr_b16 v[92:93], v59 offset:16384\n\t"
      "ds_read_b64_tr_b16 v[94:95], v59 offset:17408\n\t"
      "v_mfma_f32_32x32x16_bf16 v[96:111], v[72:75], v[180:183], v[96:111]\n\t"
      "v_mfma_f32_32x32x16_bf16 v[112:127], v[76:79], v[180:183], v[112:127]\n\t"
      "v_smfmac_f32_16x16x64_bf16 v[48:51], v[40:43], v[64:71], v44\n\t"
      "s_waitcnt vmcnt(0) lgkmcnt(0)\n\t"
      "s_barrier\n\t"
      "v_mfma_f32_32x32x16_bf16 v[96:111], v[80:83], v[184:187], v[96:111]\n\t"
      "ds_read_b64_tr_b16 v[64:65], v32 offset:32768\n\t"
      "ds_read_b64_tr_b16 v[66:67], v32 offset:33792\n\t"
      "ds_read_b64_tr_b16 v[68:69], v36 offset:32768\n\t"
      "ds_read_b64_tr_b16 v[70:71], v36 offset:33792\n\t"
      "v_mfma_f32_32x32x16_bf16 v[112:127], v[84:87], v[184:187], v[112:127]\n\t"
      "ds_read_b64_tr_b16 v[72:73], v33 offset:32768\n\t"
      "ds_read_b64_tr_b16 v[74:75], v33 offset:33792\n\t"
      "ds_read_b64_tr_b16 v[76:77], v37 offset:32768\n\t"
      "ds_read_b64_tr_b16 v[78:79], v37 offset:33792\n\t"
      "v_mfma_f32_32x32x16_bf16 v[96:111], v[88:91], v[188:191], v[96:111]\n\t"
      "v_mfma_f32_32x32x16_bf16 v[112:127], v[92:95], v[188:191], v[112:127]\n\t"
      "s_waitcnt lgkmcnt(0)\n\t"
      "v_mfma_f32_32x32x16_bf16 v[96:111], v[64:67], v[192:195], v[96:111]\n\t"
      "ds_read_b64_tr_b16 v[80:81], v34 offset:32768\n\t"
      "ds_read_b64_tr_b16 v[82:83], v34 offset:33792\n\t"
      "ds_read_b64_tr_b16 v[84:85], v38 offset:32768\n\t"
      "ds_read_b64_tr_b16 v[86:87], v38 offset:33792\n\t"
      "v_mfma_f32_32x32x16_bf16 v[112:127], v[68:71], v[192:195], v[112:127]\n\t"
      "ds_read_b64_tr_b16 v[88:89], v35 offset:32768\n\t"
      "ds_read_b64_tr_b16 v[90:91], v35 offset:33792\n\t"
      "ds_read_b64_tr_b16 v[92:93], v39 offset:32768\n\t"
      "ds_read_b64_tr_b16 v[94:95], v39 offset:33792\n\t"
      "v_mfma_f32_32x32x16_bf16 v[96:111], v[72:75], v[196:199], v[96:111]\n\t"
      "v_mfma_f32_32x32x16_bf16 v[112:127], v[76:79], v[196:199], v[112:127]\n\t"
      "v_smfmac_f32_16x16x64_bf16 v[48:51], v[40:43], v[64:71], v44\n\t"
      "s_waitcnt lgkmcnt(0)\n\t"
      "v_mfma_f32_32x32x16_bf16 v[96:111], v[80:83], v[200:203], v[96:111]\n\t"
      "ds_read_b64_tr_b16 v[64:65], v52 offset:32768\n\t"
      "ds_read_b64_tr_b16 v[66:67], v52 offset:33792\n\t"
      "ds_read_b64_tr_b16 v[68:69], v56 offset:32768\n\t"
      "ds_read_b64_tr_b16 v[70:71], v56 offset:33792\n\t"
      "v_mfma_f32_32x32x16_bf16 v[112:127], v[84:87], v[200:203], v[112:127]\n\t"
      "ds_read_b64_tr_b16 v[72:73], v53 offset:32768\n\t"
      "ds_read_b64_tr_b16 v[74:75], v53 offset:33792\n\t"
      "ds_read_b64_tr_b16 v[76:77], v57 offset:32768\n\t"
      "ds_read_b64_tr_b16 v[78:79], v57 offset:33792\n\t"
      "v_mfma_f32_32x32x16_bf16 v[96:111], v[88:91], v[204:207], v[96:111]\n\t"
      "v_mfma_f32_32x32x16_bf16 v[112:127], v[92:95], v[204:207], v[112:127]\n\t"
      "s_waitcnt lgkmcnt(0)\n\t"
      "v_mfma_f32_32x32x16_bf16 v[96:111], v[64:67], v[208:211], v[96:111]\n\t"
      "ds_read_b64_tr_b16 v[80:81], v54 offset:32768\n\t"
      "ds_read_b64_tr_b16 v[82:83], v54 offset:33792\n\t"
      "ds_read_b64_tr_b16 v[84:85], v58 offset:32768\n\t"
      "ds_read_b64_tr_b16 v[86:87], v58 offset:33792\n\t"
      "v_mfma_f32_32x32x16_bf16 v[112:127], v[68:71], v[208:211], v[112:127]\n\t"
      "ds_read_b64_tr_b16 v[88:89], v55 offset:32768\n\t"
      "ds_read_b64_tr_b16 v[90:91], v55 offset:33792\n\t"
      "ds_read_b64_tr_b16 v[92:93], v59 offset:32768\n\t"
      "ds_read_b64_tr_b16 v[94:95], v59 offset:33792\n\t"
      "v_mfma_f32_32x32x16_bf16 v[96:111], v[72:75], v[212:215], v[96:111]\n\t"
      "v_mfma_f32_32x32x16_bf16 v[112:127], v[76:79], v[212:215], v[112:127]\n\t"
      "v_smfmac_f32_16x16x64_bf16 v[48:51], v[40:43], v[64:71], v44\n\t"
      "s_waitcnt vmcnt(0) lgkmcnt(0)\n\t"
      "s_barrier\n\t"
      "v_mfma_f32_32x32x16_bf16 v[96:111], v[80:83], v[216:219], v[96:111]\n\t"
      "ds_read_b64_tr_b16 v[64:65], v32 offset:49152\n\t"
      "ds_read_b64_tr_b16 v[66:67], v32 offset:50176\n\t"
      "ds_read_b64_tr_b16 v[68:69], v36 offset:49152\n\t"
      "ds_read_b64_tr_b16 v[70:71], v36 offset:50176\n\t"
      "v_mfma_f32_32x32x16_bf16 v[112:127], v[84:87], v[216:219], v[112:127]\n\t"
      "ds_read_b64_tr_b16 v[72:73], v33 offset:49152\n\t"
      "ds_read_b64_tr_b16 v[74:75], v33 offset:50176\n\t"
      "ds_read_b64_tr_b16 v[76:77], v37 offset:49152\n\t"
      "ds_read_b64_tr_b16 v[78:79], v37 offset:50176\n\t"
      "v_mfma_f32_32x32x16_bf16 v[96:111], v[88:91], v[220:223], v[96:111]\n\t"
      "v_mfma_f32_32x32x16_bf16 v[112:127], v[92:95], v[220:223], v[112:127]\n\t"
      "s_waitcnt lgkmcnt(0)\n\t"
      "v_mfma_f32_32x32x16_bf16 v[96:111], v[64:67], v[224:227], v[96:111]\n\t"
      "ds_read_b64_tr_b16 v[80:81], v34 offset:49152\n\t"
      "ds_read_b64_tr_b16 v[82:83], v34 offset:50176\n\t"
      "ds_read_b64_tr_b16 v[84:85], v38 offset:49152\n\t"
      "ds_read_b64_tr_b16 v[86:87], v38 offset:50176\n\t"
      "v_mfma_f32_32x32x16_bf16 v[112:127], v[68:71], v[224:227], v[112:127]\n\t"
      "ds_read_b64_tr_b16 v[88:89], v35 offset:49152\n\t"
      "ds_read_b64_tr_b16 v[90:91], v35 offset:50176\n\t"
      "ds_read_b64_tr_b16 v[92:93], v39 offset:49152\n\t"
      "ds_read_b64_tr_b16 v[94:95], v39 offset:50176\n\t"
      "v_mfma_f32_32x32x16_bf16 v[96:111], v[72:75], v[228:231], v[96:111]\n\t"
      "v_mfma_f32_32x32x16_bf16 v[112:127], v[76:79], v[228:231], v[112:127]\n\t"
      "v_smfmac_f32_16x16x64_bf16 v[48:51], v[40:43], v[64:71], v44\n\t"
      "s_waitcnt lgkmcnt(0)\n\t"
      "v_mfma_f32_32x32x16_bf16 v[96:111], v[80:83], v[232:235], v[96:111]\n\t"
      "ds_read_b64_tr_b16 v[64:65], v52 offset:49152\n\t"
      "ds_read_b64_tr_b16 v[66:67], v52 offset:50176\n\t"
      "ds_read_b64_tr_b16 v[68:69], v56 offset:49152\n\t"
      "ds_read_b64_tr_b16 v[70:71], v56 offset:50176\n\t"
      "v_mfma_f32_32x32x16_bf16 v[112:127], v[84:87], v[232:235], v[112:127]\n\t"
      "ds_read_b64_tr_b16 v[72:73], v53 offset:49152\n\t"
      "ds_read_b64_tr_b16 v[74:75], v53 offset:50176\n\t"
      "ds_read_b64_tr_b16 v[76:77], v57 offset:49152\n\t"
      "ds_read_b64_tr_b16 v[78:79], v57 offset:50176\n\t"
      "v_mfma_f32_32x32x16_bf16 v[96:111], v[88:91], v[236:239], v[96:111]\n\t"
      "v_mfma_f32_32x32x16_bf16 v[112:127], v[92:95], v[236:239], v[112:127]\n\t"
      "s_waitcnt lgkmcnt(0)\n\t"
      "v_mfma_f32_32x32x16_bf16 v[96:111], v[64:67], v[240:243], v[96:111]\n\t"
      "ds_read_b64_tr_b16 v[80:81], v54 offset:49152\n\t"
      "ds_read_b64_tr_b16 v[82:83], v54 offset:50176\n\t"
      "ds_read_b64_tr_b16 v[84:85], v58 offset:49152\n\t"
      "ds_read_b64_tr_b16 v[86:87], v58 offset:50176\n\t"
      "v_mfma_f32_32x32x16_bf16 v[112:127], v[68:71], v[240:243], v[112:127]\n\t"
      "ds_read_b64_tr_b16 v[88:89], v55 offset:49152\n\t"
      "ds_read_b64_tr_b16 v[90:91], v55 offset:50176\n\t"
      "ds_read_b64_tr_b16 v[92:93], v59 offset:49152\n\t"
      "ds_read_b64_tr_b16 v[94:95], v59 offset:50176\n\t"
      "v_mfma_f32_32x32x16_bf16 v[96:111], v[72:75], v[244:247], v[96:111]\n\t"
      "v_mfma_f32_32x32x16_bf16 v[112:127], v[76:79], v[244:247], v[112:127]\n\t"
      "v_smfmac_f32_16x16x64_bf16 v[48:51], v[40:43], v[64:71], v44\n\t"
      "s_waitcnt lgkmcnt(0)\n\t"
      "v_mfma_f32_32x32x16_bf16 v[96:111], v[80:83], v[248:251], v[96:111]\n\t"
      "v_mfma_f32_32x32x16_bf16 v[112:127], v[84:87], v[248:251], v[112:127]\n\t"
      "v_mfma_f32_32x32x16_bf16 v[96:111], v[88:91], v[252:255], v[96:111]\n\t"
      "v_mfma_f32_32x32x16_bf16 v[112:127], v[92:95], v[252:255], v[112:127]\n\t"
      "s_mov_b32 m0, s68\n\t"
      "s_nop 7\n\t"
      "v_add_f32 v60, v48, v49\n\t"
      "s_nop 7\n\t"
      "s_branch 21f\n\t20:\n\t"
      "s_mov_b32 s68, m0\n\t"
      "s_mov_b64 s[70:71], %[xs]\n\t"
      "s_mov_b64 s[72:73], %[qs]\n\t"
      "s_mov_b32 s69, 0x3f803f80\n\t"
      "v_xor_b32 v48, 0, %[xb]\n\t"
      "v_add_u32 v28, 0x10000, v48\n\t"
      "v_xor_b32 v52, 0, %[wb0]\n\t"
      "v_xor_b32 v56, 0, %[wb1]\n\t"
      "v_xor_b32 v49, 32, %[xb]\n\t"
      "v_add_u32 v29, 0x10000, v49\n\t"
      "v_xor_b32 v53, 4096, %[wb0]\n\t"
      "v_xor_b32 v57, 4096, %[wb1]\n\t"
      "v_xor_b32 v50, 64, %[xb]\n\t"
      "v_add_u32 v30, 0x10000, v50\n\t"
      "v_xor_b32 v54, 8192, %[wb0]\n\t"
      "v_xor_b32 v58, 8192, %[wb1]\n\t"
      "v_xor_b32 v51, 96, %[xb]\n\t"
      "v_add_u32 v31, 0x10000, v51\n\t"
      "v_xor_b32 v55, 12288, %[wb0]\n\t"
      "v_xor_b32 v59, 12288, %[wb1]\n\t"
      "v_subrev_u32 v32, 0x10000, v52\n\t"
      "v_subrev_u32 v36, 0x10000, v56\n\t"
      "v_subrev_u32 v33, 0x10000, v53\n\t"
      "v_subrev_u32 v37, 0x10000, v57\n\t"
      "v_subrev_u32 v34, 0x10000, v54\n\t"
      "v_subrev_u32 v38, 0x10000, v58\n\t"
      "v_subrev_u32 v35, 0x10000, v55\n\t"
      "v_subrev_u32 v39, 0x10000, v59\n\t"
      "v_mov_b32 v40, %[calo]\n\t"
      "v_mov_b32 v41, %[calo]\n\t"
      "v_mov_b32 v42, %[cahi]\n\t"
      "v_mov_b32 v43, %[cahi]\n\t"
      "v_mov_b32 v44, %[cidx]\n\t"
      "v_mov_b32 v64, 0\n\t"
      "v_mov_b32 v65, 0\n\t"
      "v_mov_b32 v66, 0\n\t"
      "v_mov_b32 v67, 0\n\t"
      "s_waitcnt vmcnt(1)\n\t"
      "s_barrier\n\t"
      "ds_read_b128 v[128:131], v48 offset:0\n\t"
      "ds_read_b128 v[132:135], v49 offset:0\n\t"
      "ds_read_b128 v[136:139], v50 offset:0\n\t"
      "ds_read_b128 v[140:143], v51 offset:0\n\t"
      "ds_read_b128 v[144:147], v28 offset:0\n\t"
      "ds_read_b128 v[148:151], v29 offset:0\n\t"
      "ds_read_b128 v[152:155], v30 offset:0\n\t"
      "ds_read_b128 v[156:159], v31 offset:0\n\t"
      "s_waitcnt lgkmcnt(7)\n\t"
      "v_dot2c_f32_bf16 v64, s69, v128\n\t"
      "v_dot2c_f32_bf16 v65, v128, v128\n\t"
      "v_dot2c_f32_bf16 v66, s69, v129\n\t"
      "v_dot2c_f32_bf16 v67, v129, v129\n\t"
      "v_dot2c_f32_bf16 v64, s69, v130\n\t"
      "v_dot2c_f32_bf16 v65, v130, v130\n\t"
      "v_dot2c_f32_bf16 v66, s69, v131\n\t"
      "v_dot2c_f32_bf16 v67, v131, v131\n\t"
      "s_waitcnt lgkmcnt(6)\n\t"
      "v_dot2c_f32_bf16 v64, s69, v132\n\t"
      "v_dot2c_f32_bf16 v65, v132, v132\n\t"
      "v_dot2c_f32_bf16 v66, s69, v133\n\t"
      "v_dot2c_f32_bf16 v67, v133, v133\n\t"
      "v_dot2c_f32_bf16 v64, s69, v134\n\t"
      "v_dot2c_f32_bf16 v65, v134, v134\n\t"
      "v_dot2c_f32_bf16 v66, s69, v135\n\t"
      "v_dot2c_f32_bf16 v67, v135, v135\n\t"
      "s_waitcnt lgkmcnt(5)\n\t"
      "v_dot2c_f32_bf16 v64, s69, v136\n\t"
      "v_dot2c_f32_bf16 v65, v136, v136\n\t"
      "v_dot2c_f32_bf16 v66, s69, v137\n\t"
      "v_dot2c_f32_bf16 v67, v137, v137\n\t"
      "v_dot2c_f32_bf16 v64, s69, v138\n\t"
      "v_dot2c_f32_bf16 v65, v138, v138\n\t"
      "v_dot2c_f32_bf16 v66, s69, v139\n\t"
      "v_dot2c_f32_bf16 v67, v139, v139\n\t"
      "s_waitcnt lgkmcnt(4)\n\t"
      "v_dot2c_f32_bf16 v64, s69, v140\n\t"
      "v_dot2c_f32_bf16 v65, v140, v140\n\t"
      "v_dot2c_f32_bf16 v66, s69, v141\n\t"
      "v_dot2c_f32_bf16 v67, v141, v141\n\t"
      "v_dot2c_f32_bf16 v64, s69, v142\n\t"
      "v_dot2c_f32_bf16 v65, v142, v142\n\t"
      "v_dot2c_f32_bf16 v66, s69, v143\n\t"
      "v_dot2c_f32_bf16 v67, v143, v143\n\t"
      "s_waitcnt lgkmcnt(3)\n\t"
      "v_dot2c_f32_bf16 v64, s69, v144\n\t"
      "v_dot2c_f32_bf16 v65, v144, v144\n\t"
      "v_dot2c_f32_bf16 v66, s69, v145\n\t"
      "v_dot2c_f32_bf16 v67, v145, v145\n\t"
      "v_dot2c_f32_bf16 v64, s69, v146\n\t"
      "v_dot2c_f32_bf16 v65, v146, v146\n\t"
      "v_dot2c_f32_bf16 v66, s69, v147\n\t"
      "v_dot2c_f32_bf16 v67, v147, v147\n\t"
      "s_waitcnt lgkmcnt(2)\n\t"
      "v_dot2c_f32_bf16 v64, s69, v148\n\t"
      "v_dot2c_f32_bf16 v65, v148, v148\n\t"
      "v_dot2c_f32_bf16 v66, s69, v149\n\t"
      "v_dot2c_f32_bf16 v67, v149, v149\n\t"
      "v_dot2c_f32_bf16 v64, s69, v150\n\t"
      "v_dot2c_f32_bf16 v65, v150, v150\n\t"
      "v_dot2c_f32_bf16 v66, s69, v151\n\t"
      "v_dot2c_f32_bf16 v67, v151, v151\n\t"
      "s_waitcnt lgkmcnt(1)\n\t"
      "v_dot2c_f32_bf16 v64, s69, v152\n\t"
      "v_dot2c_f32_bf16 v65, v152, v152\n\t"
      "v_dot2c_f32_bf16 v66, s69, v153\n\t"
      "v_dot2c_f32_bf16 v67, v153, v153\n\t"
      "v_dot2c_f32_bf16 v64, s69, v154\n\t"
      "v_dot2c_f32_bf16 v65, v154, v154\n\t"
      "v_dot2c_f32_bf16 v66, s69, v155\n\t"
      "v_dot2c_f32_bf16 v67, v155, v155\n\t"
      "s_waitcnt lgkmcnt(0)\n\t"
      "v_dot2c_f32_bf16 v64, s69, v156\n\t"
      "v_dot2c_f32_bf16 v65, v156, v156\n\t"
      "v_dot2c_f32_bf16 v66, s69, v157\n\t"
      "v_dot2c_f32_bf16 v67, v157, v157\n\t"
      "v_dot2c_f32_bf16 v64, s69, v158\n\t"
      "v_dot2c_f32_bf16 v65, v158, v158\n\t"
      "v_dot2c_f32_bf16 v66, s69, v159\n\t"
      "v_dot2c_f32_bf16 v67, v159, v159\n\t"
      "s_barrier\n\t"
      "ds_read_b128 v[160:163], v48 offset:16384\n\t"
      "ds_read_b128 v[164:167], v49 offset:16384\n\t"
      "ds_read_b128 v[168:171], v50 offset:16384\n\t"
      "ds_read_b128 v[172:175], v51 offset:16384\n\t"
      "ds_read_b128 v[176:179], v28 offset:16384\n\t"
      "ds_read_b128 v[180:183], v29 offset:16384\n\t"
      "ds_read_b128 v[184:187], v30 offset:16384\n\t"
      "ds_read_b128 v[188:191], v31 offset:16384\n\t"
      "s_waitcnt lgkmcnt(7)\n\t"
      "v_dot2c_f32_bf16 v64, s69, v160\n\t"
      "v_dot2c_f32_bf16 v65, v160, v160\n\t"
      "v_dot2c_f32_bf16 v66, s69, v161\n\t"
      "v_dot2c_f32_bf16 v67, v161, v161\n\t"
      "v_dot2c_f32_bf16 v64, s69, v162\n\t"
      "v_dot2c_f32_bf16 v65, v162, v162\n\t"
      "v_dot2c_f32_bf16 v66, s69, v163\n\t"
      "v_dot2c_f32_bf16 v67, v163, v163\n\t"
      "s_waitcnt lgkmcnt(6)\n\t"
      "v_dot2c_f32_bf16 v64, s69, v164\n\t"
      "v_dot2c_f32_bf16 v65, v164, v164\n\t"
      "v_dot2c_f32_bf16 v66, s69, v165\n\t"
      "v_dot2c_f32_bf16 v67, v165, v165\n\t"
      "v_dot2c_f32_bf16 v64, s69, v166\n\t"
      "v_dot2c_f32_bf16 v65, v166, v166\n\t"
      "v_dot2c_f32_bf16 v66, s69, v167\n\t"
      "v_dot2c_f32_bf16 v67, v167, v167\n\t"
      "s_waitcnt lgkmcnt(5)\n\t"
      "v_dot2c_f32_bf16 v64, s69, v168\n\t"
      "v_dot2c_f32_bf16 v65, v168, v168\n\t"
      "v_dot2c_f32_bf16 v66, s69, v169\n\t"
      "v_dot2c_f32_bf16 v67, v169, v169\n\t"
      "v_dot2c_f32_bf16 v64, s69, v170\n\t"
      "v_dot2c_f32_bf16 v65, v170, v170\n\t"
      "v_dot2c_f32_bf16 v66, s69, v171\n\t"
      "v_dot2c_f32_bf16 v67, v171, v171\n\t"
      "s_waitcnt lgkmcnt(4)\n\t"
      "v_dot2c_f32_bf16 v64, s69, v172\n\t"
      "v_dot2c_f32_bf16 v65, v172, v172\n\t"
      "v_dot2c_f32_bf16 v66, s69, v173\n\t"
      "v_dot2c_f32_bf16 v67, v173, v173\n\t"
      "v_dot2c_f32_bf16 v64, s69, v174\n\t"
      "v_dot2c_f32_bf16 v65, v174, v174\n\t"
      "v_dot2c_f32_bf16 v66, s69, v175\n\t"
      "v_dot2c_f32_bf16 v67, v175, v175\n\t"
      "s_waitcnt lgkmcnt(3)\n\t"
      "v_dot2c_f32_bf16 v64, s69, v176\n\t"
      "v_dot2c_f32_bf16 v65, v176, v176\n\t"
      "v_dot2c_f32_bf16 v66, s69, v177\n\t"
      "v_dot2c_f32_bf16 v67, v177, v177\n\t"
      "v_dot2c_f32_bf16 v64, s69, v178\n\t"
      "v_dot2c_f32_bf16 v65, v178, v178\n\t"
      "v_dot2c_f32_bf16 v66, s69, v179\n\t"
      "v_dot2c_f32_bf16 v67, v179, v179\n\t"
      "s_waitcnt lgkmcnt(2)\n\t"
      "v_dot2c_f32_bf16 v64, s69, v180\n\t"
      "v_dot2c_f32_bf16 v65, v180, v180\n\t"
      "v_dot2c_f32_bf16 v66, s69, v181\n\t"
      "v_dot2c_f32_bf16 v67, v181, v181\n\t"
      "v_dot2c_f32_bf16 v64, s69, v182\n\t"
      "v_dot2c_f32_bf16 v65, v182, v182\n\t"
      "v_dot2c_f32_bf16 v66, s69, v183\n\t"
      "v_dot2c_f32_bf16 v67, v183, v183\n\t"
      "s_waitcnt lgkmcnt(1)\n\t"
      "v_dot2c_f32_bf16 v64, s69, v184\n\t"
      "v_dot2c_f32_bf16 v65, v184, v184\n\t"
      "v_dot2c_f32_bf16 v66, s69, v185\n\t"
      "v_dot2c_f32_bf16 v67, v185, v185\n\t"
      "v_dot2c_f32_bf16 v64, s69, v186\n\t"
      "v_dot2c_f32_bf16 v65, v186, v186\n\t"
      "v_dot2c_f32_bf16 v66, s69, v187\n\t"
      "v_dot2c_f32_bf16 v67, v187, v187\n\t"
      "s_waitcnt lgkmcnt(0)\n\t"
      "v_dot2c_f32_bf16 v64, s69, v188\n\t"
      "v_dot2c_f32_bf16 v65, v188, v188\n\t"
      "v_dot2c_f32_bf16 v66, s69, v189\n\t"
      "v_dot2c_f32_bf16 v67, v189, v189\n\t"
      "v_dot2c_f32_bf16 v64, s69, v190\n\t"
      "v_dot2c_f32_bf16 v65, v190, v190\n\t"
      "v_dot2c_f32_bf16 v66, s69, v191\n\t"
      "v_dot2c_f32_bf16 v67, v191, v191\n\t"
      "s_barrier\n\t"
      "ds_read_b128 v[192:195], v48 offset:32768\n\t"
      "ds_read_b128 v[196:199], v49 offset:32768\n\t"
      "ds_read_b128 v[200:203], v50 offset:32768\n\t"
      "ds_read_b128 v[204:207], v51 offset:32768\n\t"
      "ds_read_b128 v[208:211], v28 offset:32768\n\t"
      "ds_read_b128 v[212:215], v29 offset:32768\n\t"
      "ds_read_b128 v[216:219], v30 offset:32768\n\t"
      "ds_read_b128 v[220:223], v31 offset:32768\n\t"
      "s_waitcnt lgkmcnt(7)\n\t"
      "v_dot2c_f32_bf16 v64, s69, v192\n\t"
      "v_dot2c_f32_bf16 v65, v192, v192\n\t"
      "v_dot2c_f32_bf16 v66, s69, v193\n\t"
      "v_dot2c_f32_bf16 v67, v193, v193\n\t"
      "v_dot2c_f32_bf16 v64, s69, v194\n\t"
      "v_dot2c_f32_bf16 v65, v194, v194\n\t"
      "v_dot2c_f32_bf16 v66, s69, v195\n\t"
      "v_dot2c_f32_bf16 v67, v195, v195\n\t"
      "s_waitcnt lgkmcnt(6)\n\t"
      "v_dot2c_f32_bf16 v64, s69, v196\n\t"
      "v_dot2c_f32_bf16 v65, v196, v196\n\t"
      "v_dot2c_f32_bf16 v66, s69, v197\n\t"
      "v_dot2c_f32_bf16 v67, v197, v197\n\t"
      "v_dot2c_f32_bf16 v64, s69, v198\n\t"
      "v_dot2c_f32_bf16 v65, v198, v198\n\t"
      "v_dot2c_f32_bf16 v66, s69, v199\n\t"
      "v_dot2c_f32_bf16 v67, v199, v199\n\t"
      "s_waitcnt lgkmcnt(5)\n\t"
      "v_dot2c_f32_bf16 v64, s69, v200\n\t"
      "v_dot2c_f32_bf16 v65, v200, v200\n\t"
      "v_dot2c_f32_bf16 v66, s69, v201\n\t"
      "v_dot2c_f32_bf16 v67, v201, v201\n\t"
      "v_dot2c_f32_bf16 v64, s69, v202\n\t"
      "v_dot2c_f32_bf16 v65, v202, v202\n\t"
      "v_dot2c_f32_bf16 v66, s69, v203\n\t"
      "v_dot2c_f32_bf16 v67, v203, v203\n\t"
      "s_waitcnt lgkmcnt(4)\n\t"
      "v_dot2c_f32_bf16 v64, s69, v204\n\t"
      "v_dot2c_f32_bf16 v65, v204, v204\n\t"
      "v_dot2c_f32_bf16 v66, s69, v205\n\t"
      "v_dot2c_f32_bf16 v67, v205, v205\n\t"
      "v_dot2c_f32_bf16 v64, s69, v206\n\t"
      "v_dot2c_f32_bf16 v65, v206, v206\n\t"
      "v_dot2c_f32_bf16 v66, s69, v207\n\t"
      "v_dot2c_f32_bf16 v67, v207, v207\n\t"
      "s_waitcnt lgkmcnt(3)\n\t"
      "v_dot2c_f32_bf16 v64, s69, v208\n\t"
      "v_dot2c_f32_bf16 v65, v208, v208\n\t"
      "v_dot2c_f32_bf16 v66, s69, v209\n\t"
      "v_dot2c_f32_bf16 v67, v209, v209\n\t"
      "v_dot2c_f32_bf16 v64, s69, v210\n\t"
      "v_dot2c_f32_bf16 v65, v210, v210\n\t"
      "v_dot2c_f32_bf16 v66, s69, v211\n\t"
      "v_dot2c_f32_bf16 v67, v211, v211\n\t"
      "s_waitcnt lgkmcnt(2)\n\t"
      "v_dot2c_f32_bf16 v64, s69, v212\n\t"
      "v_dot2c_f32_bf16 v65, v212, v212\n\t"
      "v_dot2c_f32_bf16 v66, s69, v213\n\t"
      "v_dot2c_f32_bf16 v67, v213, v213\n\t"
      "v_dot2c_f32_bf16 v64, s69, v214\n\t"
      "v_dot2c_f32_bf16 v65, v214, v214\n\t"
      "v_dot2c_f32_bf16 v66, s69, v215\n\t"
      "v_dot2c_f32_bf16 v67, v215, v215\n\t"
      "s_waitcnt lgkmcnt(1)\n\t"
      "v_dot2c_f32_bf16 v64, s69, v216\n\t"
      "v_dot2c_f32_bf16 v65, v216, v216\n\t"
      "v_dot2c_f32_bf16 v66, s69, v217\n\t"
      "v_dot2c_f32_bf16 v67, v217, v217\n\t"
      "v_dot2c_f32_bf16 v64, s69, v218\n\t"
      "v_dot2c_f32_bf16 v65, v218, v218\n\t"
      "v_dot2c_f32_bf16 v66, s69, v219\n\t"
      "v_dot2c_f32_bf16 v67, v219, v219\n\t"
      "s_waitcnt lgkmcnt(0)\n\t"
      "v_dot2c_f32_bf16 v64, s69, v220\n\t"
      "v_dot2c_f32_bf16 v65, v220, v220\n\t"
      "v_dot2c_f32_bf16 v66, s69, v221\n\t"
      "v_dot2c_f32_bf16 v67, v221, v221\n\t"
      "v_dot2c_f32_bf16 v64, s69, v222\n\t"
      "v_dot2c_f32_bf16 v65, v222, v222\n\t"
      "v_dot2c_f32_bf16 v66, s69, v223\n\t"
      "v_dot2c_f32_bf16 v67, v223, v223\n\t"
      "s_barrier\n\t"
      "ds_read_b128 v[224:227], v48 offset:49152\n\t"
      "ds_read_b128 v[228:231], v49 offset:49152\n\t"
      "ds_read_b128 v[232:235], v50 offset:49152\n\t"
      "ds_read_b128 v[236:239], v51 offset:49152\n\t"
      "ds_read_b128 v[240:243], v28 offset:49152\n\t"
      "ds_read_b128 v[244:247], v29 offset:49152\n\t"
      "ds_read_b128 v[248:251], v30 offset:49152\n\t"
      "ds_read_b128 v[252:255], v31 offset:49152\n\t"
      "s_waitcnt lgkmcnt(7)\n\t"
      "v_dot2c_f32_bf16 v64, s69, v224\n\t"
      "v_dot2c_f32_bf16 v65, v224, v224\n\t"
      "v_dot2c_f32_bf16 v66, s69, v225\n\t"
      "v_dot2c_f32_bf16 v67, v225, v225\n\t"
      "v_dot2c_f32_bf16 v64, s69, v226\n\t"
      "v_dot2c_f32_bf16 v65, v226, v226\n\t"
      "v_dot2c_f32_bf16 v66, s69, v227\n\t"
      "v_dot2c_f32_bf16 v67, v227, v227\n\t"
      "s_add_u32 s64, s72, 786432\n\t"
      "s_addc_u32 s65, s73, 0\n\t"
      "s_add_u32 m0, %[dst4], 32768\n\t"
      "s_nop 0\n\t"
      "global_load_lds_dwordx4 %[vq0], s[64:65]\n\t"
      "s_waitcnt lgkmcnt(6)\n\t"
      "v_dot2c_f32_bf16 v64, s69, v228\n\t"
      "v_dot2c_f32_bf16 v65, v228, v228\n\t"
      "v_dot2c_f32_bf16 v66, s69, v229\n\t"
      "v_dot2c_f32_bf16 v67, v229, v229\n\t"
      "v_dot2c_f32_bf16 v64, s69, v230\n\t"
      "v_dot2c_f32_bf16 v65, v230, v230\n\t"
      "v_dot2c_f32_bf16 v66, s69, v231\n\t"
      "v_dot2c_f32_bf16 v67, v231, v231\n\t"
      "global_load_lds_dwordx4 %[vq1], s[64:65] offset:1024\n\t"
      "s_waitcnt lgkmcnt(5)\n\t"
      "v_dot2c_f32_bf16 v64, s69, v232\n\t"
      "v_dot2c_f32_bf16 v65, v232, v232\n\t"
      "v_dot2c_f32_bf16 v66, s69, v233\n\t"
      "v_dot2c_f32_bf16 v67, v233, v233\n\t"
      "v_dot2c_f32_bf16 v64, s69, v234\n\t"
      "v_dot2c_f32_bf16 v65, v234, v234\n\t"
      "v_dot2c_f32_bf16 v66, s69, v235\n\t"
      "v_dot2c_f32_bf16 v67, v235, v235\n\t"
      "s_add_u32 s64, s64, 24576\n\t"
      "s_addc_u32 s65, s65, 0\n\t"
      "s_add_u32 m0, %[dst4], 34816\n\t"
      "s_nop 0\n\t"
      "global_load_lds_dwordx4 %[vq0], s[64:65]\n\t"
      "s_waitcnt lgkmcnt(4)\n\t"
      "v_dot2c_f32_bf16 v64, s69, v236\n\t"
      "v_dot2c_f32_bf16 v65, v236, v236\n\t"
      "v_dot2c_f32_bf16 v66, s69, v237\n\t"
      "v_dot2c_f32_bf16 v67, v237, v237\n\t"
      "v_dot2c_f32_bf16 v64, s69, v238\n\t"
      "v_dot2c_f32_bf16 v65, v238, v238\n\t"
      "v_dot2c_f32_bf16 v66, s69, v239\n\t"
      "v_dot2c_f32_bf16 v67, v239, v239\n\t"
      "global_load_lds_dwordx4 %[vq1], s[64:65] offset:1024\n\t"
      "s_waitcnt lgkmcnt(3)\n\t"
      "v_dot2c_f32_bf16 v64, s69, v240\n\t"
      "v_dot2c_f32_bf16 v65, v240, v240\n\t"
      "v_dot2c_f32_bf16 v66, s69, v241\n\t"
      "v_dot2c_f32_bf16 v67, v241, v241\n\t"
      "v_dot2c_f32_bf16 v64, s69, v242\n\t"
      "v_dot2c_f32_bf16 v65, v242, v242\n\t"
      "v_dot2c_f32_bf16 v66, s69, v243\n\t"
      "v_dot2c_f32_bf16 v67, v243, v243\n\t"
      "s_add_u32 s64, s72, 983040\n\t"
      "s_addc_u32 s65, s73, 0\n\t"
      "s_add_u32 m0, %[dst4], 98304\n\t"
      "s_nop 0\n\t"
      "global_load_lds_dwordx4 %[vq0], s[64:65]\n\t"
      "s_waitcnt lgkmcnt(2)\n\t"
      "v_dot2c_f32_bf16 v64, s69, v244\n\t"
      "v_dot2c_f32_bf16 v65, v244, v244\n\t"
      "v_dot2c_f32_bf16 v66, s69, v245\n\t"
      "v_dot2c_f32_bf16 v67, v245, v245\n\t"
      "v_dot2c_f32_bf16 v64, s69, v246\n\t"
      "v_dot2c_f32_bf16 v65, v246, v246\n\t"
      "v_dot2c_f32_bf16 v66, s69, v247\n\t"
      "v_dot2c_f32_bf16 v67, v247, v247\n\t"
      "global_load_lds_dwordx4 %[vq1], s[64:65] offset:1024\n\t"
      "s_waitcnt lgkmcnt(1)\n\t"
      "v_dot2c_f32_bf16 v64, s69, v248\n\t"
      "v_dot2c_f32_bf16 v65, v248, v248\n\t"
      "v_dot2c_f32_bf16 v66, s69, v249\n\t"
      "v_dot2c_f32_bf16 v67, v249, v249\n\t"
      "v_dot2c_f32_bf16 v64, s69, v250\n\t"
      "v_dot2c_f32_bf16 v65, v250, v250\n\t"
      "v_dot2c_f32_bf16 v66, s69, v251\n\t"
      "v_dot2c_f32_bf16 v67, v251, v251\n\t"
      "s_add_u32 s64, s64, 24576\n\t"
      "s_addc_u32 s65, s65, 0\n\t"
      "s_add_u32 m0, %[dst4], 100352\n\t"
      "s_nop 0\n\t"
      "global_load_lds_dwordx4 %[vq0], s[64:65]\n\t"
      "s_waitcnt lgkmcnt(0)\n\t"
      "v_dot2c_f32_bf16 v64, s69, v252\n\t"
      "v_dot2c_f32_bf16 v65, v252, v252\n\t"
      "v_dot2c_f32_bf16 v66, s69, v253\n\t"
      "v_dot2c_f32_bf16 v67, v253, v253\n\t"
      "v_dot2c_f32_bf16 v64, s69, v254\n\t"
      "v_dot2c_f32_bf16 v65, v254, v254\n\t"
      "v_dot2c_f32_bf16 v66, s69, v255\n\t"
      "v_dot2c_f32_bf16 v67, v255, v255\n\t"
      "global_load_lds_dwordx4 %[vq1], s[64:65] offset:1024\n\t"
      "s_nop 2\n\t"
      "v_add_f32 v62, v64, v66\n\t"
      "v_add_f32 v63, v65, v67\n\t"
      "s_barrier\n\t"
      "ds_read_b64_tr_b16 v[64:65], v32 offset:0\n\t"
      "ds_read_b64_tr_b16 v[66:67], v32 offset:1024\n\t"
      "ds_read_b64_tr_b16 v[68:69], v33 offset:0\n\t"
      "ds_read_b64_tr_b16 v[70:71], v33 offset:1024\n\t"
      "v_mov_b32 v48, 0\n\t"
      "v_mov_b32 v49, 0\n\t"
      "v_mov_b32 v50, 0\n\t"
      "v_mov_b32 v51, 0\n\t"
      "s_waitcnt lgkmcnt(0)\n\t"
      "v_mfma_f32_32x32x16_bf16 v[96:111], v[64:67], v[128:131], 0\n\t"
      "ds_read_b64_tr_b16 v[80:81], v34 offset:0\n\t"
      "ds_read_b64_tr_b16 v[82:83], v34 offset:1024\n\t"
      "ds_read_b64_tr_b16 v[84:85], v35 offset:0\n\t"
      "ds_read_b64_tr_b16 v[86:87], v35 offset:1024\n\t"
      "s_add_u32 s64, s72, 1179648\n\t"
      "s_addc_u32 s65, s73, 0\n\t"
      "s_add_u32 m0, %[dst4], 49152\n\t"
      "s_nop 0\n\t"
      "global_load_lds_dwordx4 %[vq0], s[64:65]\n\t"
      "v_mfma_f32_32x32x16_bf16 v[96:111], v[68:71], v[132:135], v[96:111]\n\t"
      "global_load_lds_dwordx4 %[vq1], s[64:65] offset:1024\n\t"
      "v_smfmac_f32_16x16x64_bf16 v[48:51], v[40:43], v[64:71], v44\n\t"
      "s_waitcnt lgkmcnt(0)\n\t"
      "v_mfma_f32_32x32x16_bf16 v[96:111], v[80:83], v[136:139], v[96:111]\n\t"
      "ds_read_b64_tr_b16 v[64:65], v52 offset:0\n\t"
      "ds_read_b64_tr_b16 v[66:67], v52 offset:1024\n\t"
      "ds_read_b64_tr_b16 v[68:69], v53 offset:0\n\t"
      "ds_read_b64_tr_b16 v[70:71], v53 offset:1024\n\t"
      "s_add_u32 s64, s64, 24576\n\t"
      "s_addc_u32 s65, s65, 0\n\t"
      "s_add_u32 m0, %[dst4], 51200\n\t"
      "s_nop 0\n\t"
      "global_load_lds_dwordx4 %[vq0], s[64:65]\n\t"
      "v_mfma_f32_32x32x16_bf16 v[96:111], v[84:87], v[140:143], v[96:111]\n\t"
      "global_load_lds_dwordx4 %[vq1], s[64:65] offset:1024\n\t"
      "s_add_u32 s64, s72, 1376256\n\t"
      "s_addc_u32 s65, s73, 0\n\t"
      "s_add_u32 m0, %[dst4], 114688\n\t"
      "s_nop 0\n\t"
      "global_load_lds_dwordx4 %[vq0], s[64:65]\n\t"
      "s_waitcnt lgkmcnt(0)\n\t"
      "v_mfma_f32_32x32x16_bf16 v[96:111], v[64:67], v[144:147], v[96:111]\n\t"
      "ds_read_b64_tr_b16 v[80:81], v54 offset:0\n\t"
      "ds_read_b64_tr_b16 v[82:83], v54 offset:1024\n\t"
      "ds_read_b64_tr_b16 v[84:85], v55 offset:0\n\t"
      "ds_read_b64_tr_b16 v[86:87], v55 offset:1024\n\t"
      "global_load_lds_dwordx4 %[vq1], s[64:65] offset:1024\n\t"
      "v_mfma_f32_32x32x16_bf16 v[96:111], v[68:71], v[148:151], v[96:111]\n\t"
      "s_add_u32 s64, s64, 24576\n\t"
      "s_addc_u32 s65, s65, 0\n\t"
      "s_add_u32 m0, %[dst4], 116736\n\t"
      "s_nop 0\n\t"
      "global_load_lds_dwordx4 %[vq0], s[64:65]\n\t"
      "global_load_lds_dwordx4 %[vq1], s[64:65] offset:1024\n\t"
      "v_smfmac_f32_16x16x64_bf16 v[48:51], v[40:43], v[64:71], v44\n\t"
      "s_waitcnt lgkmcnt(0)\n\t"
      "s_barrier\n\t"
      "v_mfma_f32_32x32x16_bf16 v[96:111], v[80:83], v[152:155], v[96:111]\n\t"
      "ds_read_b64_tr_b16 v[64:65], v32 offset:16384\n\t"
      "ds_read_b64_tr_b16 v[66:67], v32 offset:17408\n\t"
      "ds_read_b64_tr_b16 v[68:69], v33 offset:16384\n\t"
      "ds_read_b64_tr_b16 v[70:71], v33 offset:17408\n\t"
      "s_mov_b64 s[64:65], %[p0]\n\t"
      "s_add_u32 m0, %[dst4], 0\n\t"
      "s_nop 0\n\t"
      "global_load_lds_dwordx4 %[vw0], s[64:65]\n\t"
      "v_mfma_f32_32x32x16_bf16 v[96:111], v[84:87], v[156:159], v[96:111]\n\t"
      "global_load_lds_dwordx4 %[vw1], s[64:65] offset:1024\n\t"
      "s_waitcnt lgkmcnt(0)\n\t"
      "v_mfma_f32_32x32x16_bf16 v[96:111], v[64:67], v[160:163], v[96:111]\n\t"
      "ds_read_b64_tr_b16 v[80:81], v34 offset:16384\n\t"
      "ds_read_b64_tr_b16 v[82:83], v34 offset:17408\n\t"
      "ds_read_b64_tr_b16 v[84:85], v35 offset:16384\n\t"
      "ds_read_b64_tr_b16 v[86:87], v35 offset:17408\n\t"
      "s_add_u32 s64, s64, %[wst]\n\t"
      "s_addc_u32 s65, s65, 0\n\t"
      "s_add_u32 m0, %[dst4], 2048\n\t"
      "s_nop 0\n\t"
      "global_load_lds_dwordx4 %[vw0], s[64:65]\n\t"
      "v_mfma_f32_32x32x16_bf16 v[96:111], v[68:71], v[164:167], v[96:111]\n\t"
      "global_load_lds_dwordx4 %[vw1], s[64:65] offset:1024\n\t"
      "v_smfmac_f32_16x16x64_bf16 v[48:51], v[40:43], v[64:71], v44\n\t"
      "s_waitcnt lgkmcnt(0)\n\t"
      "v_mfma_f32_32x32x16_bf16 v[96:111], v[80:83], v[168:171], v[96:111]\n\t"
      "ds_read_b64_tr_b16 v[64:65], v52 offset:16384\n\t"
      "ds_read_b64_tr_b16 v[66:67], v52 offset:17408\n\t"
      "ds_read_b64_tr_b16 v[68:69], v53 offset:16384\n\t"
      "ds_read_b64_tr_b16 v[70:71], v53 offset:17408\n\t"
      "s_mov_b64 s[64:65], %[p1]\n\t"
      "s_add_u32 m0, %[dst4], 65536\n\t"
      "s_nop 0\n\t"
      "global_load_lds_dwordx4 %[vw0], s[64:65]\n\t"
      "v_mfma_f32_32x32x16_bf16 v[96:111], v[84:87], v[172:175], v[96:111]\n\t"
      "global_load_lds_dwordx4 %[vw1], s[64:65] offset:1024\n\t"
      "s_waitcnt lgkmcnt(0)\n\t"
      "v_mfma_f32_32x32x16_bf16 v[96:111], v[64:67], v[176:179], v[96:111]\n\t"
      "ds_read_b64_tr_b16 v[80:81], v54 offset:16384\n\t"
      "ds_read_b64_tr_b16 v[82:83], v54 offset:17408\n\t"
      "ds_read_b64_tr_b16 v[84:85], v55 offset:16384\n\t"
      "ds_read_b64_tr_b16 v[86:87], v55 offset:17408\n\t"
      "s_add_u32 s64, s64, %[wst]\n\t"
      "s_addc_u32 s65, s65, 0\n\t"
      "s_add_u32 m0, %[dst4], 67584\n\t"
      "s_nop 0\n\t"
      "global_load_lds_dwordx4 %[vw0], s[64:65]\n\t"
      "v_mfma_f32_32x32x16_bf16 v[96:111], v[68:71], v[180:183], v[96:111]\n\t"
      "global_load_lds_dwordx4 %[vw1], s[64:65] offset:1024\n\t"
      "v_smfmac_f32_16x16x64_bf16 v[48:51], v[40:43], v[64:71], v44\n\t"
      "s_waitcnt vmcnt(16) lgkmcnt(0)\n\t"
      "s_barrier\n\t"
      "v_mfma_f32_32x32x16_bf16 v[96:111], v[80:83], v[184:187], v[96:111]\n\t"
      "ds_read_b64_tr_b16 v[64:65], v32 offset:32768\n\t"
      "ds_read_b64_tr_b16 v[66:67], v32 offset:33792\n\t"
      "ds_read_b64_tr_b16 v[68:69], v33 offset:32768\n\t"
      "ds_read_b64_tr_b16 v[70:71], v33 offset:33792\n\t"
      "s_mov_b64 s[64:65], %[p2]\n\t"
      "s_add_u32 m0, %[dst4], 16384\n\t"
      "s_nop 0\n\t"
      "global_load_lds_dwordx4 %[vw0], s[64:65]\n\t"
      "v_mfma_f32_32x32x16_bf16 v[96:111], v[84:87], v[188:191], v[96:111]\n\t"
      "global_load_lds_dwordx4 %[vw1], s[64:65] offset:1024\n\t"
      "s_waitcnt lgkmcnt(0)\n\t"
      "v_mfma_f32_32x32x16_bf16 v[96:111], v[64:67], v[192:195], v[96:111]\n\t"
      "ds_read_b64_tr_b16 v[80:81], v34 offset:32768\n\t"
      "ds_read_b64_tr_b16 v[82:83], v34 offset:33792\n\t"
      "ds_read_b64_tr_b16 v[84:85], v35 offset:32768\n\t"
      "ds_read_b64_tr_b16 v[86:87], v35 offset:33792\n\t"
      "s_add_u32 s64, s64, %[wst]\n\t"
      "s_addc_u32 s65, s65, 0\n\t"
      "s_add_u32 m0, %[dst4], 18432\n\t"
      "s_nop 0\n\t"
      "global_load_lds_dwordx4 %[vw0], s[64:65]\n\t"
      "v_mfma_f32_32x32x16_bf16 v[96:111], v[68:71], v[196:199], v[96:111]\n\t"
      "global_load_lds_dwordx4 %[vw1], s[64:65] offset:1024\n\t"
      "v_smfmac_f32_16x16x64_bf16 v[48:51], v[40:43], v[64:71], v44\n\t"
      "s_waitcnt lgkmcnt(0)\n\t"
      "v_mfma_f32_32x32x16_bf16 v[96:111], v[80:83], v[200:203], v[96:111]\n\t"
      "ds_read_b64_tr_b16 v[64:65], v52 offset:32768\n\t"
      "ds_read_b64_tr_b16 v[66:67], v52 offset:33792\n\t"
      "ds_read_b64_tr_b16 v[68:69], v53 offset:32768\n\t"
      "ds_read_b64_tr_b16 v[70:71], v53 offset:33792\n\t"
      "s_mov_b64 s[64:65], %[p3]\n\t"
      "s_add_u32 m0, %[dst4], 81920\n\t"
      "s_nop 0\n\t"
      "global_load_lds_dwordx4 %[vw0], s[64:65]\n\t"
      "v_mfma_f32_32x32x16_bf16 v[96:111], v[84:87], v[204:207], v[96:111]\n\t"
      "global_load_lds_dwordx4 %[vw1], s[64:65] offset:1024\n\t"
      "s_waitcnt lgkmcnt(0)\n\t"
      "v_mfma_f32_32x32x16_bf16 v[96:111], v[64:67], v[208:211], v[96:111]\n\t"
      "ds_read_b64_tr_b16 v[80:81], v54 offset:32768\n\t"
      "ds_read_b64_tr_b16 v[82:83], v54 offset:33792\n\t"
      "ds_read_b64_tr_b16 v[84:85], v55 offset:32768\n\t"
      "ds_read_b64_tr_b16 v[86:87], v55 offset:33792\n\t"
      "s_add_u32 s64, s64, %[wst]\n\t"
      "s_addc_u32 s65, s65, 0\n\t"
      "s_add_u32 m0, %[dst4], 83968\n\t"
      "s_nop 0\n\t"
      "global_load_lds_dwordx4 %[vw0], s[64:65]\n\t"
      "v_mfma_f32_32x32x16_bf16 v[96:111], v[68:71], v[212:215], v[96:111]\n\t"
      "global_load_lds_dwordx4 %[vw1], s[64:65] offset:1024\n\t"
      "v_smfmac_f32_16x16x64_bf16 v[48:51], v[40:43], v[64:71], v44\n\t"
      "s_waitcnt vmcnt(16) lgkmcnt(0)\n\t"
      "s_barrier\n\t"
      "v_mfma_f32_32x32x16_bf16 v[96:111], v[80:83], v[216:219], v[96:111]\n\t"
      "ds_read_b64_tr_b16 v[64:65], v32 offset:49152\n\t"
      "ds_read_b64_tr_b16 v[66:67], v32 offset:50176\n\t"
      "ds_read_b64_tr_b16 v[68:69], v33 offset:49152\n\t"
      "ds_read_b64_tr_b16 v[70:71], v33 offset:50176\n\t"
      "s_mov_b64 s[64:65], %[p4]\n\t"
      "s_add_u32 m0, %[dst4], 32768\n\t"
      "s_nop 0\n\t"
      "global_load_lds_dwordx4 %[vw0], s[64:65]\n\t"
      "v_mfma_f32_32x32x16_bf16 v[96:111], v[84:87], v[220:223], v[96:111]\n\t"
      "global_load_lds_dwordx4 %[vw1], s[64:65] offset:1024\n\t"
      "s_waitcnt lgkmcnt(0)\n\t"
      "v_mfma_f32_32x32x16_bf16 v[96:111], v[64:67], v[224:227], v[96:111]\n\t"
      "ds_read_b64_tr_b16 v[80:81], v34 offset:49152\n\t"
      "ds_read_b64_tr_b16 v[82:83], v34 offset:50176\n\t"
      "ds_read_b64_tr_b16 v[84:85], v35 offset:49152\n\t"
      "ds_read_b64_tr_b16 v[86:87], v35 offset:50176\n\t"
      "s_add_u32 s64, s64, %[wst]\n\t"
      "s_addc_u32 s65, s65, 0\n\t"
      "s_add_u32 m0, %[dst4], 34816\n\t"
      "s_nop 0\n\t"
      "global_load_lds_dwordx4 %[vw0], s[64:65]\n\t"
      "v_mfma_f32_32x32x16_bf16 v[96:111], v[68:71], v[228:231], v[96:111]\n\t"
      "global_load_lds_dwordx4 %[vw1], s[64:65] offset:1024\n\t"
      "v_smfmac_f32_16x16x64_bf16 v[48:51], v[40:43], v[64:71], v44\n\t"
      "s_waitcnt lgkmcnt(0)\n\t"
      "v_mfma_f32_32x32x16_bf16 v[96:111], v[80:83], v[232:235], v[96:111]\n\t"
      "ds_read_b64_tr_b16 v[64:65], v52 offset:49152\n\t"
      "ds_read_b64_tr_b16 v[66:67], v52 offset:50176\n\t"
      "ds_read_b64_tr_b16 v[68:69], v53 offset:49152\n\t"
      "ds_read_b64_tr_b16 v[70:71], v53 offset:50176\n\t"
      "s_mov_b64 s[64:65], %[p5]\n\t"
      "s_add_u32 m0, %[dst4], 98304\n\t"
      "s_nop 0\n\t"
      "global_load_lds_dwordx4 %[vw0], s[64:65]\n\t"
      "v_mfma_f32_32x32x16_bf16 v[96:111], v[84:87], v[236:239], v[96:111]\n\t"
      "global_load_lds_dwordx4 %[vw1], s[64:65] offset:1024\n\t"
      "s_waitcnt lgkmcnt(0)\n\t"
      "v_mfma_f32_32x32x16_bf16 v[96:111], v[64:67], v[240:243], v[96:111]\n\t"
      "ds_read_b64_tr_b16 v[80:81], v54 offset:49152\n\t"
      "ds_read_b64_tr_b16 v[82:83], v54 offset:50176\n\t"
      "ds_read_b64_tr_b16 v[84:85], v55 offset:49152\n\t"
      "ds_read_b64_tr_b16 v[86:87], v55 offset:50176\n\t"
      "s_add_u32 s64, s64, %[wst]\n\t"
      "s_addc_u32 s65, s65, 0\n\t"
      "s_add_u32 m0, %[dst4], 100352\n\t"
      "s_nop 0\n\t"
      "global_load_lds_dwordx4 %[vw0], s[64:65]\n\t"
      "v_mfma_f32_32x32x16_bf16 v[96:111], v[68:71], v[244:247], v[96:111]\n\t"
      "global_load_lds_dwordx4 %[vw1], s[64:65] offset:1024\n\t"
      "v_smfmac_f32_16x16x64_bf16 v[48:51], v[40:43], v[64:71], v44\n\t"
      "s_waitcnt lgkmcnt(0)\n\t"
      "v_mfma_f32_32x32x16_bf16 v[96:111], v[80:83], v[248:251], v[96:111]\n\t"
      "v_mfma_f32_32x32x16_bf16 v[96:111], v[84:87], v[252:255], v[96:111]\n\t"
      "s_mov_b32 m0, s68\n\t"
      "s_nop 7\n\t"
      "v_add_f32 v60, v48, v49\n\t"
      "s_nop 7\n\t"
      "21:\n\t"
      : "=&{v[128:159]}"(xf0), "=&{v[160:191]}"(xf1), "=&{v[192:223]}"(xf2), "=&{v[224:255]}"(xf3), "=&{v[96:127]}"(acc), "=&{v[60:63]}"(st)
      : [xb] "v"(xb), [wb0] "v"(wb0), [wb1] "v"(wb1), [vx0] "v"(vx0), [vx1] "v"(vx1), [vq0] "v"(vq0), [vq1] "v"(vq1), [vw0] "v"(VW0(2 * m)), [vw1] "v"(VW1(2 * m)),
        [calo] "v"(CSEL(0)), [cahi] "v"(CSEL(1)), [cidx] "v"(CIDX),
        [xs] "s"(xs), [qs] "s"(qs), [dst4] "s"(m * 4096), [wst] "s"(8 * st2), [wv] "s"(w),
        [p0] "s"(p2src(0)), [p1] "s"(p2src(1)), [p2] "s"(p2src(2)), [p3] "s"(p2src(3)), [p4] "s"(p2src(4)), [p5] "s"(p2src(5))
      : "v28", "v29", "v30", "v31", "v32", "v33", "v34", "v35", "v36", "v37", "v38", "v39", "v40", "v41", "v42", "v43", "v44", "v45", "v46", "v47", "v48", "v49", "v50", "v51", "v52", "v53", "v54", "v55", "v56", "v57", "v58", "v59", "v64", "v65", "v66", "v67", "v68", "v69", "v70", "v71", "v72", "v73", "v74", "v75", "v76", "v77", "v78", "v79", "v80", "v81", "v82", "v83", "v84", "v85", "v86", "v87", "v88", "v89", "v90", "v91", "v92", "v93", "v94", "v95", "s64", "s65", "s68", "s69", "s70", "s71", "s72", "s73", "scc", "memory");
  }
  RELANE();
  STAMP(1);
  LAS float* rowp = (LAS float*)(lds + MISC);
  LAS float* colp = (LAS float*)(lds + MISC + 2048);
  LAS float* colp2 = (LAS float*)(lds + MISC + 4096);
  LAS float* priv = (LAS float*)(lds + MISC + 6144 + w * 512);
  LAS unsigned* cnt_h = (LAS unsigned*)(lds + CNT);
  if (g2) {
    const float mean = swap_add(st[2]) * (1.f / 512.f);
    const float rs = __builtin_amdgcn_rsqf(fmaxf(swap_add(st[3]) * (1.f / 512.f) - mean * mean, 0.f) + 1e-5f);
    rowp[(32 * m + t) * 2 + h] = h ? mean * rs : rs;
    st[0] = swap_add(st[0]) * 0.5f;
  }
  colp[m * 128 + g2 * 64 + l] = st[0];
  WAIT_BAR(16);
  STAMP(2);
  u64 tag;
  {
    const u32x2 e = *(LAS u32x2*)(lds + MISC + 10496);
    tag = (((u64)__builtin_amdgcn_readfirstlane(e[1]) << 32) | (unsigned)__builtin_amdgcn_readfirstlane(e[0])) + 1;
  }
  const f32x2 rowst = *(LAS f32x2*)(rowp + (32 * m + t) * 2);
  const float rstd = rowst[0], mur = rowst[1];
  priv[l] = colp[g2 * 64 + l] + colp[128 + g2 * 64 + l] + colp[256 + g2 * 64 + l] + colp[384 + g2 * 64 + l];
  const int ttile = rb * 4 + m;
  if (g2) {
    ST_DEV((u32x4{~0u, ~0u, ~0u, ~0u}), WS_O + ((qb * 8 + 2 * qp) * 32 + head) * 1024 + l * 16);
    ST_DEV((u32x4{~0u, ~0u, ~0u, ~0u}), WS_O + ((qb * 8 + 2 * qp + 1) * 32 + head) * 1024 + l * 16);
  }
#pragma unroll
  for (int j = 0; j < 2; ++j) {
    if (j == 0 || g2 == 0) {
      const int tau = 3 * cb + 2 * g2 + j, hp = tau & 15;
      float y[16];
      const float sc = tau < 16 ? 0.25f * 1.4426950408889634f : 1.0f;
      const float rs = rstd * sc, mm = -mur * sc;
#pragma unroll
      for (int g = 0; g < 4; ++g) {
        f32x4 c = *(LAS f32x4*)(priv + j * 32 + 8 * g + 4 * h);
#pragma unroll
        for (int e = 0; e < 4; ++e) y[4 * g + e] = fmaf(acc[16 * j + 4 * g + e], rs, mm * c[e]);
      }
      u32x4 o0 = {pk2(y[0], y[1]), pk2(y[2], y[3]), pk2(y[4], y[5]), pk2(y[6], y[7])};
      u32x4 o1 = {pk2(y[8], y[9]), pk2(y[10], y[11]), pk2(y[12], y[13]), pk2(y[14], y[15])};
      unsigned doff = tau < 16 ? WS_Q + ((2 * hp) * 64 + ttile) * 1024 + l * 16 : WS_KV + ((2 * hp) * 64 + ttile) * 2048 + l * 16;
      unsigned dstep = tau < 16 ? 65536 : 131072;
      if (tau >= 32) {
        ldsp vs = lds + VSCR + w * 2048;
        *(LAS u32x4*)(vs + t * 32 + h * 16) = o0;
        *(LAS u32x4*)(vs + 1024 + t * 32 + h * 16) = o1;
        ldsp rp = vs + ((l >> 4) & 1) * 1024 + (8 * h + q4) * 32 + (l & 1) * 16 + ((l >> 1) & 1) * 8;
        o0 = lds_tr2(rp, rp + 128);
        o1 = lds_tr2(rp + 512, rp + 640);
        doff = WS_KV + ((2 * hp + (t >> 4)) * 64 + ttile) * 2048 + 1024 + h * 256 + (t & 15) * 16;
        dstep = 512;
      }
      ST_DEV(o0, doff);
      ST_DEV(o1, doff + dstep);
    }
  }
  const int cbk = (16 + (head >> 1)) / 3, cbv = (32 + (head >> 1)) / 3, cbq = (head >> 1) / 3;
#define POLLQ (WS_FQ + ((l < 32 ? (l & 15) : 2 * qb + (l & 1)) * 16 + (l < 16 ? cbk : (l < 32 ? cbv : cbq))) * 64)
  const unsigned myflag = (rb * 16 + cb) * 64;
  const int kt0 = kh * 32 + qp;
  const char* qsrc = ws + WS_Q + (head * 64 + qb * 8 + w) * 1024;
  const char* kvsrc = ws + WS_KV + head * 131072 + kt0 * 2048;
  auto load_kv = [&](int c0) {
#pragma unroll
    for (int c = c0; c < 8; ++c) glds2_dev(kvsrc + c * 8192, l * 16, (kt0 + 4 * c) * KVSTRIDE);
    if (w < 2) { const unsigned v = w ? 0u : 0x3f803f80u; *(LAS u32x4*)(lds + l * KVSTRIDE + 2048 + w * 16) = u32x4{v, v, v, v}; }
    if (w == 2) cnt_h[l] = 0;
  };
  STAMP(3);
  if (!tr_role) {
    {
      const unsigned wb0 = WLANE + (((2 * g2) ^ q4) << 6), wb1 = WLANE + (((2 * g2 + 1) ^ q4) << 6);
      asm volatile(
      "s_mov_b32 s68, m0\n\t"
      "s_mov_b64 s[64:65], %[kvs]\n\t"
      "v_xor_b32 v52, 0, %[wb0]\n\t"
      "v_xor_b32 v56, 0, %[wb1]\n\t"
      "v_xor_b32 v53, 4096, %[wb0]\n\t"
      "v_xor_b32 v57, 4096, %[wb1]\n\t"
      "v_xor_b32 v54, 8192, %[wb0]\n\t"
      "v_xor_b32 v58, 8192, %[wb1]\n\t"
      "v_xor_b32 v55, 12288, %[wb0]\n\t"
      "v_xor_b32 v59, 12288, %[wb1]\n\t"
      "v_subrev_u32 v32, 0x10000, v52\n\t"
      "v_subrev_u32 v36, 0x10000, v56\n\t"
      "v_subrev_u32 v33, 0x10000, v53\n\t"
      "v_subrev_u32 v37, 0x10000, v57\n\t"
      "v_subrev_u32 v34, 0x10000, v54\n\t"
      "v_subrev_u32 v38, 0x10000, v58\n\t"
      "v_subrev_u32 v35, 0x10000, v55\n\t"
      "v_subrev_u32 v39, 0x10000, v59\n\t"
      "v_mov_b32 v40, %[calo]\n\t"
      "v_mov_b32 v41, %[calo]\n\t"
      "v_mov_b32 v42, %[cahi]\n\t"
      "v_mov_b32 v43, %[cahi]\n\t"
      "v_mov_b32 v44, %[cidx]\n\t"
      "ds_read_b64_tr_b16 v[64:65], v32 offset:0\n\t"
      "ds_read_b64_tr_b16 v[66:67], v32 offset:1024\n\t"
      "ds_read_b64_tr_b16 v[68:69], v36 offset:0\n\t"
      "ds_read_b64_tr_b16 v[70:71], v36 offset:1024\n\t"
      "ds_read_b64_tr_b16 v[72:73], v33 offset:0\n\t"
      "ds_read_b64_tr_b16 v[74:75], v33 offset:1024\n\t"
      "ds_read_b64_tr_b16 v[76:77], v37 offset:0\n\t"
      "ds_read_b64_tr_b16 v[78:79], v37 offset:1024\n\t"
      "v_mov_b32 v48, 0\n\t"
      "v_mov_b32 v49, 0\n\t"
      "v_mov_b32 v50, 0\n\t"
      "v_mov_b32 v51, 0\n\t"
      "s_waitcnt lgkmcnt(0)\n\t"
      "v_mfma_f32_32x32x16_bf16 v[96:111], v[64:67], v[128:131], 0\n\t"
      "ds_read_b64_tr_b16 v[80:81], v34 offset:0\n\t"
      "ds_read_b64_tr_b16 v[82:83], v34 offset:1024\n\t"
      "ds_read_b64_tr_b16 v[84:85], v38 offset:0\n\t"
      "ds_read_b64_tr_b16 v[86:87], v38 offset:1024\n\t"
      "v_mfma_f32_32x32x16_bf16 v[112:127], v[68:71], v[128:131], 0\n\t"
      "ds_read_b64_tr_b16 v[88:89], v35 offset:0\n\t"
      "ds_read_b64_tr_b16 v[90:91], v35 offset:1024\n\t"
      "ds_read_b64_tr_b16 v[92:93], v39 offset:0\n\t"
      "ds_read_b64_tr_b16 v[94:95], v39 offset:1024\n\t"
      "s_add_u32 m0, %[dstw], 49152\n\t"
      "s_nop 0\n\t"
      "global_load_lds_dwordx4 %[vw0], %[p6]\n\t"
      "v_mfma_f32_32x32x16_bf16 v[96:111], v[72:75], v[132:135], v[96:111]\n\t"
      "v_mfma_f32_32x32x16_bf16 v[112:127], v[76:79], v[132:135], v[112:127]\n\t"
      "global_load_lds_dwordx4 %[vw1], %[p6] offset:1024\n\t"
      "v_smfmac_f32_16x16x64_bf16 v[48:51], v[40:43], v[64:71], v44\n\t"
      "s_waitcnt lgkmcnt(0)\n\t"
      "v_mfma_f32_32x32x16_bf16 v[96:111], v[80:83], v[136:139], v[96:111]\n\t"
      "ds_read_b64_tr_b16 v[64:65], v52 offset:0\n\t"
      "ds_read_b64_tr_b16 v[66:67], v52 offset:1024\n\t"
      "ds_read_b64_tr_b16 v[68:69], v56 offset:0\n\t"
      "ds_read_b64_tr_b16 v[70:71], v56 offset:1024\n\t"
      "v_mfma_f32_32x32x16_bf16 v[112:127], v[84:87], v[136:139], v[112:127]\n\t"
      "ds_read_b64_tr_b16 v[72:73], v53 offset:0\n\t"
      "ds_read_b64_tr_b16 v[74:75], v53 offset:1024\n\t"
      "ds_read_b64_tr_b16 v[76:77], v57 offset:0\n\t"
      "ds_read_b64_tr_b16 v[78:79], v57 offset:1024\n\t"
      "s_add_u32 m0, %[dstw], 114688\n\t"
      "s_nop 0\n\t"
      "global_load_lds_dwordx4 %[vw0], %[p7]\n\t"
      "v_mfma_f32_32x32x16_bf16 v[96:111], v[88:91], v[140:143], v[96:111]\n\t"
      "v_mfma_f32_32x32x16_bf16 v[112:127], v[92:95], v[140:143], v[112:127]\n\t"
      "global_load_lds_dwordx4 %[vw1], %[p7] offset:1024\n\t"
      "s_waitcnt lgkmcnt(0)\n\t"
      "v_mfma_f32_32x32x16_bf16 v[96:111], v[64:67], v[144:147], v[96:111]\n\t"
      "ds_read_b64_tr_b16 v[80:81], v54 offset:0\n\t"
      "ds_read_b64_tr_b16 v[82:83], v54 offset:1024\n\t"
      "ds_read_b64_tr_b16 v[84:85], v58 offset:0\n\t"
      "ds_read_b64_tr_b16 v[86:87], v58 offset:1024\n\t"
      "v_mfma_f32_32x32x16_bf16 v[112:127], v[68:71], v[144:147], v[112:127]\n\t"
      "ds_read_b64_tr_b16 v[88:89], v55 offset:0\n\t"
      "ds_read_b64_tr_b16 v[90:91], v55 offset:1024\n\t"
      "ds_read_b64_tr_b16 v[92:93], v59 offset:0\n\t"
      "ds_read_b64_tr_b16 v[94:95], v59 offset:1024\n\t"
      "v_mfma_f32_32x32x16_bf16 v[96:111], v[72:75], v[148:151], v[96:111]\n\t"
      "v_mfma_f32_32x32x16_bf16 v[112:127], v[76:79], v[148:151], v[112:127]\n\t"
      "v_smfmac_f32_16x16x64_bf16 v[48:51], v[40:43], v[64:71], v44\n\t"
      "s_waitcnt vmcnt(4) lgkmcnt(0)\n\t"
      "s_barrier\n\t"
      "s_cmp_lg_u32 %[wv], 0\n\t"
      "s_cbranch_scc1 7f\n\t"
      "s_mov_b64 exec, 1\n\t"
      "global_store_dwordx2 %[fo], %[tag], %[ws] sc1\n\t"
      "s_mov_b64 exec, -1\n\t"
      "s_nop 4\n\t"
      "7:\n\t"
      "v_mfma_f32_32x32x16_bf16 v[96:111], v[80:83], v[152:155], v[96:111]\n\t"
      "ds_read_b64_tr_b16 v[64:65], v32 offset:16384\n\t"
      "ds_read_b64_tr_b16 v[66:67], v32 offset:17408\n\t"
      "ds_read_b64_tr_b16 v[68:69], v36 offset:16384\n\t"
      "ds_read_b64_tr_b16 v[70:71], v36 offset:17408\n\t"
      "v_mfma_f32_32x32x16_bf16 v[112:127], v[84:87], v[152:155], v[112:127]\n\t"
      "ds_read_b64_tr_b16 v[72:73], v33 offset:16384\n\t"
      "ds_read_b64_tr_b16 v[74:75], v33 offset:17408\n\t"
      "ds_read_b64_tr_b16 v[76:77], v37 offset:16384\n\t"
      "ds_read_b64_tr_b16 v[78:79], v37 offset:17408\n\t"
      "v_mfma_f32_32x32x16_bf16 v[96:111], v[88:91], v[156:159], v[96:111]\n\t"
      "v_mfma_f32_32x32x16_bf16 v[112:127], v[92:95], v[156:159], v[112:127]\n\t"
      "s_waitcnt lgkmcnt(0)\n\t"
      "v_mfma_f32_32x32x16_bf16 v[96:111], v[64:67], v[160:163], v[96:111]\n\t"
      "ds_read_b64_tr_b16 v[80:81], v34 offset:16384\n\t"
      "ds_read_b64_tr_b16 v[82:83], v34 offset:17408\n\t"
      "ds_read_b64_tr_b16 v[84:85], v38 offset:16384\n\t"
      "ds_read_b64_tr_b16 v[86:87], v38 offset:17408\n\t"
      "v_mfma_f32_32x32x16_bf16 v[112:127], v[68:71], v[160:163], v[112:127]\n\t"
      "ds_read_b64_tr_b16 v[88:89], v35 offset:16384\n\t"
      "ds_read_b64_tr_b16 v[90:91], v35 offset:17408\n\t"
      "ds_read_b64_tr_b16 v[92:93], v39 offset:16384\n\t"
      "ds_read_b64_tr_b16 v[94:95], v39 offset:17408\n\t"
      "v_mfma_f32_32x32x16_bf16 v[96:111], v[72:75], v[164:167], v[96:111]\n\t"
      "v_mfma_f32_32x32x16_bf16 v[112:127], v[76:79], v[164:167], v[112:127]\n\t"
      "v_smfmac_f32_16x16x64_bf16 v[48:51], v[40:43], v[64:71], v44\n\t"
      "s_waitcnt lgkmcnt(0)\n\t"
      "v_mfma_f32_32x32x16_bf16 v[96:111], v[80:83], v[168:171], v[96:111]\n\t"
      "ds_read_b64_tr_b16 v[64:65], v52 offset:16384\n\t"
      "ds_read_b64_tr_b16 v[66:67], v52 offset:17408\n\t"
      "ds_read_b64_tr_b16 v[68:69], v56 offset:16384\n\t"
      "ds_read_b64_tr_b16 v[70:71], v56 offset:17408\n\t"
      "v_mfma_f32_32x32x16_bf16 v[112:127], v[84:87], v[168:171], v[112:127]\n\t"
      "ds_read_b64_tr_b16 v[72:73], v53 offset:16384\n\t"
      "ds_read_b64_tr_b16 v[74:75], v53 offset:17408\n\t"
      "ds_read_b64_tr_b16 v[76:77], v57 offset:16384\n\t"
      "ds_read_b64_tr_b16 v[78:79], v57 offset:17408\n\t"
      "v_mfma_f32_32x32x16_bf16 v[96:111], v[88:91], v[172:175], v[96:111]\n\t"
      "v_mfma_f32_32x32x16_bf16 v[112:127], v[92:95], v[172:175], v[112:127]\n\t"
      "s_waitcnt lgkmcnt(0)\n\t"
      "v_mfma_f32_32x32x16_bf16 v[96:111], v[64:67], v[176:179], v[96:111]\n\t"
      "ds_read_b64_tr_b16 v[80:81], v54 offset:16384\n\t"
      "ds_read_b64_tr_b16 v[82:83], v54 offset:17408\n\t"
      "ds_read_b64_tr_b16 v[84:85], v58 offset:16384\n\t"
      "ds_read_b64_tr_b16 v[86:87], v58 offset:17408\n\t"
      "v_mfma_f32_32x32x16_bf16 v[112:127], v[68:71], v[176:179], v[112:127]\n\t"
      "ds_read_b64_tr_b16 v[88:89], v55 offset:16384\n\t"
      "ds_read_b64_tr_b16 v[90:91], v55 offset:17408\n\t"
      "ds_read_b64_tr_b16 v[92:93], v59 offset:16384\n\t"
      "ds_read_b64_tr_b16 v[94:95], v59 offset:17408\n\t"
      "v_mfma_f32_32x32x16_bf16 v[96:111], v[72:75], v[180:183], v[96:111]\n\t"
      "v_mfma_f32_32x32x16_bf16 v[112:127], v[76:79], v[180:183], v[112:127]\n\t"
      "v_smfmac_f32_16x16x64_bf16 v[48:51], v[40:43], v[64:71], v44\n\t"
      "s_waitcnt lgkmcnt(0)\n\t"
      "v_mfma_f32_32x32x16_bf16 v[96:111], v[80:83], v[184:187], v[96:111]\n\t"
      "ds_read_b64_tr_b16 v[64:65], v32 offset:32768\n\t"
      "ds_read_b64_tr_b16 v[66:67], v32 offset:33792\n\t"
      "ds_read_b64_tr_b16 v[68:69], v36 offset:32768\n\t"
      "ds_read_b64_tr_b16 v[70:71], v36 offset:33792\n\t"
      "v_mfma_f32_32x32x16_bf16 v[112:127], v[84:87], v[184:187], v[112:127]\n\t"
      "ds_read_b64_tr_b16 v[72:73], v33 offset:32768\n\t"
      "ds_read_b64_tr_b16 v[74:75], v33 offset:33792\n\t"
      "ds_read_b64_tr_b16 v[76:77], v37 offset:32768\n\t"
      "ds_read_b64_tr_b16 v[78:79], v37 offset:33792\n\t"
      "v_mfma_f32_32x32x16_bf16 v[96:111], v[88:91], v[188:191], v[96:111]\n\t"
      "v_mfma_f32_32x32x16_bf16 v[112:127], v[92:95], v[188:191], v[112:127]\n\t"
      "s_waitcnt lgkmcnt(0)\n\t"
      "v_mfma_f32_32x32x16_bf16 v[96:111], v[64:67], v[192:195], v[96:111]\n\t"
      "ds_read_b64_tr_b16 v[80:81], v34 offset:32768\n\t"
      "ds_read_b64_tr_b16 v[82:83], v34 offset:33792\n\t"
      "ds_read_b64_tr_b16 v[84:85], v38 offset:32768\n\t"
      "ds_read_b64_tr_b16 v[86:87], v38 offset:33792\n\t"
      "v_mfma_f32_32x32x16_bf16 v[112:127], v[68:71], v[192:195], v[112:127]\n\t"
      "ds_read_b64_tr_b16 v[88:89], v35 offset:32768\n\t"
      "ds_read_b64_tr_b16 v[90:91], v35 offset:33792\n\t"
      "ds_read_b64_tr_b16 v[92:93], v39 offset:32768\n\t"
      "ds_read_b64_tr_b16 v[94:95], v39 offset:33792\n\t"
      "v_mfma_f32_32x32x16_bf16 v[96:111], v[72:75], v[196:199], v[96:111]\n\t"
      "v_mfma_f32_32x32x16_bf16 v[112:127], v[76:79], v[196:199], v[112:127]\n\t"
      "v_smfmac_f32_16x16x64_bf16 v[48:51], v[40:43], v[64:71], v44\n\t"
      "s_waitcnt lgkmcnt(0)\n\t"
      "s_cmp_lg_u32 %[wv], 0\n\t"
      "s_cbranch_scc1 8f\n\t"
      "global_load_dwordx2 v[62:63], %[po], %[ws] sc1\n\t"
      "8:\n\t"
      "v_mfma_f32_32x32x16_bf16 v[96:111], v[80:83], v[200:203], v[96:111]\n\t"
      "ds_read_b64_tr_b16 v[64:65], v52 offset:32768\n\t"
      "ds_read_b64_tr_b16 v[66:67], v52 offset:33792\n\t"
      "ds_read_b64_tr_b16 v[68:69], v56 offset:32768\n\t"
      "ds_read_b64_tr_b16 v[70:71], v56 offset:33792\n\t"
      "v_mfma_f32_32x32x16_bf16 v[112:127], v[84:87], v[200:203], v[112:127]\n\t"
      "ds_read_b64_tr_b16 v[72:73], v53 offset:32768\n\t"
      "ds_read_b64_tr_b16 v[74:75], v53 offset:33792\n\t"
      "ds_read_b64_tr_b16 v[76:77], v57 offset:32768\n\t"
      "ds_read_b64_tr_b16 v[78:79], v57 offset:33792\n\t"
      "v_mfma_f32_32x32x16_bf16 v[96:111], v[88:91], v[204:207], v[96:111]\n\t"
      "v_mfma_f32_32x32x16_bf16 v[112:127], v[92:95], v[204:207], v[112:127]\n\t"
      "s_waitcnt lgkmcnt(0)\n\t"
      "v_mfma_f32_32x32x16_bf16 v[96:111], v[64:67], v[208:211], v[96:111]\n\t"
      "ds_read_b64_tr_b16 v[80:81], v54 offset:32768\n\t"
      "ds_read_b64_tr_b16 v[82:83], v54 offset:33792\n\t"
      "ds_read_b64_tr_b16 v[84:85], v58 offset:32768\n\t"
      "ds_read_b64_tr_b16 v[86:87], v58 offset:33792\n\t"
      "v_mfma_f32_32x32x16_bf16 v[112:127], v[68:71], v[208:211], v[112:127]\n\t"
      "ds_read_b64_tr_b16 v[88:89], v55 offset:32768\n\t"
      "ds_read_b64_tr_b16 v[90:91], v55 offset:33792\n\t"
      "ds_read_b64_tr_b16 v[92:93], v59 offset:32768\n\t"
      "ds_read_b64_tr_b16 v[94:95], v59 offset:33792\n\t"
      "v_mfma_f32_32x32x16_bf16 v[96:111], v[72:75], v[212:215], v[96:111]\n\t"
      "v_mfma_f32_32x32x16_bf16 v[112:127], v[76:79], v[212:215], v[112:127]\n\t"
      "v_smfmac_f32_16x16x64_bf16 v[48:51], v[40:43], v[64:71], v44\n\t"
      "s_cmp_lg_u32 %[wv], 0\n\t"
      "s_cbranch_scc1 6f\n\t"
      "s_mov_b32 s66, 0x40000\n\t"
      "5:\n\t"
      "s_waitcnt vmcnt(0)\n\t"
      "v_cmp_ne_u64 vcc, v[62:63], %[tag]\n\t"
      "s_sub_u32 s66, s66, 1\n\t"
      "s_cbranch_scc1 6f\n\t"
      "s_cbranch_vccz 6f\n\t"
      "s_sleep 2\n\t"
      "global_load_dwordx2 v[62:63], %[po], %[ws] sc1\n\t"
      "s_branch 5b\n\t"
      "6:\n\t"
      "s_waitcnt vmcnt(0) lgkmcnt(0)\n\t"
      "s_barrier\n\t"
      "v_mfma_f32_32x32x16_bf16 v[96:111], v[80:83], v[216:219], v[96:111]\n\t"
      "ds_read_b64_tr_b16 v[64:65], v32 offset:49152\n\t"
      "ds_read_b64_tr_b16 v[66:67], v32 offset:50176\n\t"
      "ds_read_b64_tr_b16 v[68:69], v36 offset:49152\n\t"
      "ds_read_b64_tr_b16 v[70:71], v36 offset:50176\n\t"
      "v_mfma_f32_32x32x16_bf16 v[112:127], v[84:87], v[216:219], v[112:127]\n\t"
      "ds_read_b64_tr_b16 v[72:73], v33 offset:49152\n\t"
      "ds_read_b64_tr_b16 v[74:75], v33 offset:50176\n\t"
      "ds_read_b64_tr_b16 v[76:77], v37 offset:49152\n\t"
      "ds_read_b64_tr_b16 v[78:79], v37 offset:50176\n\t"
      "s_mov_b32 m0, %[qd]\n\t"
      "s_nop 0\n\t"
      "global_load_lds_dwordx4 %[l16], %[qsrc] sc1\n\t"
      "v_mfma_f32_32x32x16_bf16 v[96:111], v[88:91], v[220:223], v[96:111]\n\t"
      "s_add_u32 m0, %[kvd], 0\n\t"
      "s_nop 0\n\t"
      "global_load_lds_dwordx4 %[l16], s[64:65] sc1\n\t"
      "v_mfma_f32_32x32x16_bf16 v[112:127], v[92:95], v[220:223], v[112:127]\n\t"
      "global_load_lds_dwordx4 %[l16], s[64:65] offset:1024 sc1\n\t"
      "s_add_u32 s64, s64, 8192\n\t"
      "s_addc_u32 s65, s65, 0\n\t"
      "s_waitcnt lgkmcnt(0)\n\t"
      "v_mfma_f32_32x32x16_bf16 v[96:111], v[64:67], v[224:227], v[96:111]\n\t"
      "ds_read_b64_tr_b16 v[80:81], v34 offset:49152\n\t"
      "ds_read_b64_tr_b16 v[82:83], v34 offset:50176\n\t"
      "ds_read_b64_tr_b16 v[84:85], v38 offset:49152\n\t"
      "ds_read_b64_tr_b16 v[86:87], v38 offset:50176\n\t"
      "v_mfma_f32_32x32x16_bf16 v[112:127], v[68:71], v[224:227], v[112:127]\n\t"
      "ds_read_b64_tr_b16 v[88:89], v35 offset:49152\n\t"
      "ds_read_b64_tr_b16 v[90:91], v35 offset:50176\n\t"
      "ds_read_b64_tr_b16 v[92:93], v39 offset:49152\n\t"
      "ds_read_b64_tr_b16 v[94:95], v39 offset:50176\n\t"
      "s_add_u32 m0, %[kvd], 8320\n\t"
      "s_nop 0\n\t"
      "global_load_lds_dwordx4 %[l16], s[64:65] sc1\n\t"
      "v_mfma_f32_32x32x16_bf16 v[96:111], v[72:75], v[228:231], v[96:111]\n\t"
      "v_mfma_f32_32x32x16_bf16 v[112:127], v[76:79], v[228:231], v[112:127]\n\t"
      "global_load_lds_dwordx4 %[l16], s[64:65] offset:1024 sc1\n\t"
      "s_add_u32 s64, s64, 8192\n\t"
      "s_addc_u32 s65, s65, 0\n\t"
      "v_smfmac_f32_16x16x64_bf16 v[48:51], v[40:43], v[64:71], v44\n\t"
      "s_waitcnt lgkmcnt(0)\n\t"
      "v_mfma_f32_32x32x16_bf16 v[96:111], v[80:83], v[232:235], v[96:111]\n\t"
      "ds_read_b64_tr_b16 v[64:65], v52 offset:49152\n\t"
      "ds_read_b64_tr_b16 v[66:67], v52 offset:50176\n\t"
      "ds_read_b64_tr_b16 v[68:69], v56 offset:49152\n\t"
      "ds_read_b64_tr_b16 v[70:71], v56 offset:50176\n\t"
      "v_mfma_f32_32x32x16_bf16 v[112:127], v[84:87], v[232:235], v[112:127]\n\t"
      "ds_read_b64_tr_b16 v[72:73], v53 offset:49152\n\t"
      "ds_read_b64_tr_b16 v[74:75], v53 offset:50176\n\t"
      "ds_read_b64_tr_b16 v[76:77], v57 offset:49152\n\t"
      "ds_read_b64_tr_b16 v[78:79], v57 offset:50176\n\t"
      "s_add_u32 m0, %[kvd], 16640\n\t"
      "s_nop 0\n\t"
      "global_load_lds_dwordx4 %[l16], s[64:65] sc1\n\t"
      "v_mfma_f32_32x32x16_bf16 v[96:111], v[88:91], v[236:239], v[96:111]\n\t"
      "v_mfma_f32_32x32x16_bf16 v[112:127], v[92:95], v[236:239], v[112:127]\n\t"
      "global_load_lds_dwordx4 %[l16], s[64:65] offset:1024 sc1\n\t"
      "s_add_u32 s64, s64, 8192\n\t"
      "s_addc_u32 s65, s65, 0\n\t"
      "s_waitcnt lgkmcnt(0)\n\t"
      "v_mfma_f32_32x32x16_bf16 v[96:111], v[64:67], v[240:243], v[96:111]\n\t"
      "ds_read_b64_tr_b16 v[80:81], v54 offset:49152\n\t"
      "ds_read_b64_tr_b16 v[82:83], v54 offset:50176\n\t"
      "ds_read_b64_tr_b16 v[84:85], v58 offset:49152\n\t"
      "ds_read_b64_tr_b16 v[86:87], v58 offset:50176\n\t"
      "v_mfma_f32_32x32x16_bf16 v[112:127], v[68:71], v[240:243], v[112:127]\n\t"
      "ds_read_b64_tr_b16 v[88:89], v55 offset:49152\n\t"
      "ds_read_b64_tr_b16 v[90:91], v55 offset:50176\n\t"
      "ds_read_b64_tr_b16 v[92:93], v59 offset:49152\n\t"
      "ds_read_b64_tr_b16 v[94:95], v59 offset:50176\n\t"
      "s_add_u32 m0, %[kvd], 24960\n\t"
      "s_nop 0\n\t"
      "global_load_lds_dwordx4 %[l16], s[64:65] sc1\n\t"
      "v_mfma_f32_32x32x16_bf16 v[96:111], v[72:75], v[244:247], v[96:111]\n\t"
      "v_mfma_f32_32x32x16_bf16 v[112:127], v[76:79], v[244:247], v[112:127]\n\t"
      "global_load_lds_dwordx4 %[l16], s[64:65] offset:1024 sc1\n\t"
      "s_add_u32 s64, s64, 8192\n\t"
      "s_addc_u32 s65, s65, 0\n\t"
      "v_smfmac_f32_16x16x64_bf16 v[48:51], v[40:43], v[64:71], v44\n\t"
      "s_waitcnt lgkmcnt(0)\n\t"
      "v_mfma_f32_32x32x16_bf16 v[96:111], v[80:83], v[248:251], v[96:111]\n\t"
      "v_mfma_f32_32x32x16_bf16 v[112:127], v[84:87], v[248:251], v[112:127]\n\t"
      "s_add_u32 m0, %[kvd], 33280\n\t"
      "s_nop 0\n\t"
      "global_load_lds_dwordx4 %[l16], s[64:65] sc1\n\t"
      "v_mfma_f32_32x32x16_bf16 v[96:111], v[88:91], v[252:255], v[96:111]\n\t"
      "v_mfma_f32_32x32x16_bf16 v[112:127], v[92:95], v[252:255], v[112:127]\n\t"
      "global_load_lds_dwordx4 %[l16], s[64:65] offset:1024 sc1\n\t"
      "s_add_u32 s64, s64, 8192\n\t"
      "s_addc_u32 s65, s65, 0\n\t"
      "s_mov_b32 m0, s68\n\t"
      "s_nop 7\n\t"
      "v_add_f32 v60, v48, v49\n\t"
      "s_nop 7\n\t"
        : "=&{v[96:127]}"(acc), "=&{v[60:63]}"(st)
        : "{v[128:159]}"(xf0), "{v[160:191]}"(xf1), "{v[192:223]}"(xf2), "{v[224:255]}"(xf3), [wb0] "v"(wb0), [wb1] "v"(wb1),
          [wv] "s"(w), [fo] "v"(WS_FQ + myflag), [tag] "v"(tag), [ws] "s"(ws), [po] "v"(POLLQ),
          [vw0] "v"(VW0(w)), [vw1] "v"(VW1(w)), [p6] "s"(p2src(6)), [p7] "s"(p2src(7)), [dstw] "s"(w * 2048),
          [l16] "v"(l * 16), [kvs] "s"(kvsrc), [kvd] "s"(kt0 * KVSTRIDE), [qsrc] "s"(qsrc), [qd] "s"(QSLOT + w * 1024),
          [calo] "v"(CSEL(0)), [cahi] "v"(CSEL(1)), [cidx] "v"(CIDX)
        : "v32", "v33", "v34", "v35", "v36", "v37", "v38", "v39", "v40", "v41", "v42", "v43", "v44", "v45", "v46", "v47", "v48", "v49", "v50", "v51", "v52", "v53", "v54", "v55", "v56", "v57", "v58", "v59", "v64", "v65", "v66", "v67", "v68", "v69", "v70", "v71", "v72", "v73", "v74", "v75", "v76", "v77", "v78", "v79", "v80", "v81", "v82", "v83", "v84", "v85", "v86", "v87", "v88", "v89", "v90", "v91", "v92", "v93", "v94", "v95", "s64", "s65", "s66", "s68", "vcc", "scc", "memory");
    }
    STAMP(4);
    RELANE();
    colp2[m * 128 + g2 * 64 + l] = st[0];
    WAIT_BAR(63);
    STAMP(5);
    load_kv(5);
    priv[l] = colp2[g2 * 64 + l] + colp2[128 + g2 * 64 + l] + colp2[256 + g2 * 64 + l] + colp2[384 + g2 * 64 + l];
    priv[64 + l] = bf_lo(*(LAS unsigned short*)(lds + MISC + 10240 + (g2 * 64 + l) * 2));
#pragma unroll
    for (int j = 0; j < 2; ++j) {
      float y[16];
#pragma unroll
      for (int g = 0; g < 4; ++g) {
        f32x4 c = *(LAS f32x4*)(priv + j * 32 + 8 * g + 4 * h), bb = *(LAS f32x4*)(priv + 64 + j * 32 + 8 * g + 4 * h);
#pragma unroll
        for (int e = 0; e < 4; ++e) { float v = fmaxf(fmaf(acc[16 * j + 4 * g + e], rstd, fmaf(-mur, c[e], bb[e])), 0.f); y[4 * g + e] = v * v; }
      }
      const unsigned doff = WS_H + (ttile * 112 + cb * 8 + (2 * g2 + j) * 2) * 1024 + l * 16;
      ST_DEV((u32x4{pk2(y[0], y[1]), pk2(y[2], y[3]), pk2(y[4], y[5]), pk2(y[6], y[7])}), doff);
      ST_DEV((u32x4{pk2(y[8], y[9]), pk2(y[10], y[11]), pk2(y[12], y[13]), pk2(y[14], y[15])}), doff + 1024);
    }
  } else {
    RELANE();
    WAIT_BAR(0);
    if (tid == 0) st_flag(ws, WS_FQ + myflag, tag);
    u32x4 f[5][2];
#pragma unroll
    for (int s = 0; s < 5; ++s) {
      ldsp uw = lds + (s & 1) * 65536 + (s >> 1) * 16384;
#pragma unroll
      for (int j = 0; j < 2; ++j) {
        const unsigned a = m * 4096 + h * 1024 + q4 * 256 + (((2 * g2 + j) ^ q4) << 6) + ((l >> 4) & 1) * 32 + (l & 3) * 8;
        f[s][j] = lds_tr2(uw + a, uw + a + 2048);
      }
    }
#pragma unroll
    for (int s = 0; s < 5; ++s) {
      const int u = z + 32 * s;
      if (u < 144) {
#pragma unroll
        for (int j = 0; j < 2; ++j) ST_DEV(f[s][j], WS_W + (((u & 3) * 4 + 2 * g2 + j) * 144 + (u >> 2) * 4 + m) * 1024 + l * 16);
      }
    }
    if (w == 0) poll_flags(ws, POLLQ, tag, ld_flag(ws, POLLQ));
    WAIT_BAR(63);
    STAMP(5);
    glds1_dev(qsrc, l * 16, QSLOT + w * 1024);
    load_kv(0);
    asm volatile("s_waitcnt vmcnt(0)" ::: "memory");
  }
  RELANE();
  STAMP(6);
#define VADDR(s) (t < 16 ? 1024u + (s) * 512 + h * 256 + (t & 15) * 16 : ((t == 16 || t == 28) ? 2048u : 2064u))
  int idx = 0xDDDD; PIN(idx);
  f32x16 O0, O1, F00, F01, F10, F11;
  {
    const unsigned oh0 = WS_H + (2 * tb * 112 + 14 * w) * 1024 + l * 16, ow0 = WS_W + (2 * nb * 144 + 14 * w) * 1024 + l * 16;
    const int zz = (l & 7) * 4 + (nb >> 1);
    const int ojj = ((l & 3) << 3) + (tb >> 2), oxcd = (l >> 2) & 7;
    const int oid = RB_OF(oxcd, ojj) * 16 + CB_OF(oxcd, ojj);
    const unsigned pollh = (l < 14 ? WS_FH + ((tb >> 1) * 16 + l) * 64 : (l < 32 ? WS_FH + ((zz >> 1) * 16 + 14 + (zz & 1)) * 64 : WS_FQ + oid * 64));
    const unsigned kb = kh * 32 * KVSTRIDE;
    unsigned so, cnt, ph, ka_ = kb + l * 16, va0_ = kb + VADDR(0), va1_ = kb + VADDR(1);
    asm volatile(
      "s_mov_b64 s[60:61], %[ws]\n\t"
      "s_mov_b32 s62, %[wsz]\n\t"
      "s_mov_b32 s63, 0x20000\n\t"
      "s_mov_b32 %[so], 0\n\t"
      "v_mov_b32 v112, 0\n\t"
      "v_mov_b32 v113, 0\n\t"
      "v_mov_b32 v114, 0\n\t"
      "v_mov_b32 v115, 0\n\t"
      "s_nop 1\n\t"
      "v_mfma_f32_32x32x16_bf16 %[O0], v[112:115], v[112:115], 0\n\t"
      "v_mfma_f32_32x32x16_bf16 %[O1], v[112:115], v[112:115], 0\n\t"
      "v_mfma_f32_32x32x16_bf16 %[F00], v[112:115], v[112:115], 0\n\t"
      "v_mfma_f32_32x32x16_bf16 %[F01], v[112:115], v[112:115], 0\n\t"
      "v_mfma_f32_32x32x16_bf16 %[F10], v[112:115], v[112:115], 0\n\t"
      "v_mfma_f32_32x32x16_bf16 %[F11], v[112:115], v[112:115], 0\n\t"
      "s_waitcnt vmcnt(4) lgkmcnt(0)\n\t"
      "s_barrier\n\t"
      "ds_read_b128 v[116:119], %[qa]\n\t"
      "ds_read_b128 v[108:111], %[k0]\n\t"
      "ds_read_b128 v[120:123], %[qa] offset:1024\n\t"
      "ds_read_b128 v[96:99], v156\n\t"
      "ds_read_b128 v[100:103], v157\n\t"
      "ds_read_b128 v[104:107], v158\n\t"
      "s_waitcnt lgkmcnt(3)\n\t"
      "v_mfma_f32_32x32x16_bf16 v[32:47], v[108:111], v[116:119], 0\n\t"
      "v_mfma_f32_32x32x16_bf16 v[48:63], v[108:111], v[120:123], 0\n\t"
      "s_nop 12\n\t"
      "v_max3_f32 v160, v32, v33, v34\n\t"
      "v_max3_f32 v161, v35, v36, v37\n\t"
      "v_max3_f32 v160, v160, v38, v39\n\t"
      "v_max3_f32 v161, v161, v40, v41\n\t"
      "v_max3_f32 v160, v160, v42, v43\n\t"
      "v_max3_f32 v161, v161, v44, v45\n\t"
      "v_max3_f32 v160, v160, v46, v47\n\t"
      "v_max_f32 v160, v160, v161\n\t"
      "v_mov_b32 v161, v160\n\t"
      "s_nop 1\n\t"
      "v_permlane32_swap_b32 v160, v161\n\t"
      "s_nop 1\n\t"
      "v_max_f32 v160, v160, v161\n\t"
      "v_sub_f32 v64, 0, v160\n\t"
      "v_sub_f32 v65, 0, v160\n\t"
      "v_mov_b64 v[66:67], v[64:65]\n\t"
      "v_mov_b64 v[68:69], v[64:65]\n\t"
      "v_mov_b64 v[70:71], v[64:65]\n\t"
      "v_mov_b64 v[72:73], v[64:65]\n\t"
      "v_mov_b64 v[74:75], v[64:65]\n\t"
      "v_mov_b64 v[76:77], v[64:65]\n\t"
      "v_mov_b64 v[78:79], v[64:65]\n\t"
      "s_waitcnt lgkmcnt(0)\n\t"
      "s_nop 0\n\t"
      "v_mfma_f32_32x32x16_bf16 v[32:47], v[96:99], v[116:119], v[64:79]\n\t"
      "v_max3_f32 v160, v48, v49, v50\n\t"
      "v_max3_f32 v161, v51, v52, v53\n\t"
      "v_max3_f32 v160, v160, v54, v55\n\t"
      "v_max3_f32 v161, v161, v56, v57\n\t"
      "v_max3_f32 v160, v160, v58, v59\n\t"
      "v_max3_f32 v161, v161, v60, v61\n\t"
      "v_max3_f32 v160, v160, v62, v63\n\t"
      "v_max_f32 v160, v160, v161\n\t"
      "v_mov_b32 v161, v160\n\t"
      "s_nop 1\n\t"
      "v_permlane32_swap_b32 v160, v161\n\t"
      "s_nop 1\n\t"
      "v_max_f32 v160, v160, v161\n\t"
      "v_sub_f32 v80, 0, v160\n\t"
      "v_sub_f32 v81, 0, v160\n\t"
      "v_mov_b64 v[82:83], v[80:81]\n\t"
      "v_mov_b64 v[84:85], v[80:81]\n\t"
      "v_mov_b64 v[86:87], v[80:81]\n\t"
      "v_mov_b64 v[88:89], v[80:81]\n\t"
      "v_mov_b64 v[90:91], v[80:81]\n\t"
      "v_mov_b64 v[92:93], v[80:81]\n\t"
      "v_mov_b64 v[94:95], v[80:81]\n\t"
      "v_mfma_f32_32x32x16_bf16 v[48:63], v[112:115], v[112:115], 0\n\t"
      "s_nop 12\n\t"
      "s_setprio 1\n\t"
      "s_mov_b32 %[ph], 0\n\t"
      "1:\n\t"
      "v_exp_f32 v32, v32\n\t"
      "v_exp_f32 v33, v33\n\t"
      "v_exp_f32 v34, v34\n\t"
      "v_smfmac_f32_32x32x32_bf16 %[O1], v[112:115], v[56:63], %[idx]\n\t"
      "v_exp_f32 v35, v35\n\t"
      "v_exp_f32 v36, v36\n\t"
      "v_exp_f32 v37, v37\n\t"
      "v_exp_f32 v38, v38\n\t"
      "v_mfma_f32_32x32x16_bf16 v[48:63], v[96:99], v[120:123], v[80:95]\n\t"
      "ds_read_b128 v[96:99], v156 offset:2080\n\t"
      "ds_read_b128 v[108:111], v157 offset:2080\n\t"
      "ds_read_b128 v[112:115], v158 offset:2080\n\t"
      "v_exp_f32 v39, v39\n\t"
      "v_exp_f32 v40, v40\n\t"
      "v_exp_f32 v41, v41\n\t"
      "v_exp_f32 v42, v42\n\t"
      "v_smfmac_f32_32x32x32_bf16 %[O0], v[100:103], v[32:39], %[idx]\n\t"
      "v_exp_f32 v43, v43\n\t"
      "v_exp_f32 v44, v44\n\t"
      "v_exp_f32 v45, v45\n\t"
      "v_exp_f32 v46, v46\n\t"
      "v_exp_f32 v47, v47\n\t"
      "v_exp_f32 v48, v48\n\t"
      "v_exp_f32 v49, v49\n\t"
      "v_exp_f32 v50, v50\n\t"
      "v_smfmac_f32_32x32x32_bf16 %[O0], v[104:107], v[40:47], %[idx]\n\t"
      "v_exp_f32 v51, v51\n\t"
      "v_exp_f32 v52, v52\n\t"
      "v_exp_f32 v53, v53\n\t"
      "v_exp_f32 v54, v54\n\t"
      "s_waitcnt lgkmcnt(0)\n\t"
      "v_mfma_f32_32x32x16_bf16 v[32:47], v[96:99], v[116:119], v[64:79]\n\t"
      "v_exp_f32 v55, v55\n\t"
      "v_exp_f32 v56, v56\n\t"
      "v_exp_f32 v57, v57\n\t"
      "v_exp_f32 v58, v58\n\t"
      "v_smfmac_f32_32x32x32_bf16 %[O1], v[100:103], v[48:55], %[idx]\n\t"
      "v_exp_f32 v59, v59\n\t"
      "v_exp_f32 v60, v60\n\t"
      "v_exp_f32 v61, v61\n\t"
      "s_add_u32 %[ph], %[ph], 1\n\t"
      "s_nop 1\n\t"
      "v_exp_f32 v62, v62\n\t"
      "v_exp_f32 v63, v63\n\t"
      "v_exp_f32 v32, v32\n\t"
      "v_exp_f32 v33, v33\n\t"
      "v_exp_f32 v34, v34\n\t"
      "v_smfmac_f32_32x32x32_bf16 %[O1], v[104:107], v[56:63], %[idx]\n\t"
      "v_exp_f32 v35, v35\n\t"
      "v_exp_f32 v36, v36\n\t"
      "v_exp_f32 v37, v37\n\t"
      "v_exp_f32 v38, v38\n\t"
      "v_mfma_f32_32x32x16_bf16 v[48:63], v[96:99], v[120:123], v[80:95]\n\t"
      "ds_read_b128 v[96:99], v156 offset:4160\n\t"
      "ds_read_b128 v[100:103], v157 offset:4160\n\t"
      "ds_read_b128 v[104:107], v158 offset:4160\n\t"
      "v_exp_f32 v39, v39\n\t"
      "v_exp_f32 v40, v40\n\t"
      "v_exp_f32 v41, v41\n\t"
      "v_exp_f32 v42, v42\n\t"
      "v_smfmac_f32_32x32x32_bf16 %[O0], v[108:111], v[32:39], %[idx]\n\t"
      "v_exp_f32 v43, v43\n\t"
      "v_exp_f32 v44, v44\n\t"
      "v_exp_f32 v45, v45\n\t"
      "v_exp_f32 v46, v46\n\t"
      "v_exp_f32 v47, v47\n\t"
      "v_exp_f32 v48, v48\n\t"
      "v_exp_f32 v49, v49\n\t"
      "v_exp_f32 v50, v50\n\t"
      "v_smfmac_f32_32x32x32_bf16 %[O0], v[112:115], v[40:47], %[idx]\n\t"
      "v_exp_f32 v51, v51\n\t"
      "v_exp_f32 v52, v52\n\t"
      "v_exp_f32 v53, v53\n\t"
      "v_exp_f32 v54, v54\n\t"
      "s_waitcnt lgkmcnt(0)\n\t"
      "v_mfma_f32_32x32x16_bf16 v[32:47], v[96:99], v[116:119], v[64:79]\n\t"
      "v_exp_f32 v55, v55\n\t"
      "v_exp_f32 v56, v56\n\t"
      "v_exp_f32 v57, v57\n\t"
      "v_exp_f32 v58, v58\n\t"
      "v_smfmac_f32_32x32x32_bf16 %[O1], v[108:111], v[48:55], %[idx]\n\t"
      "v_exp_f32 v59, v59\n\t"
      "v_exp_f32 v60, v60\n\t"
      "v_exp_f32 v61, v61\n\t"
      "v_add_u32 v156, 4160, v156\n\t"
      "v_add_u32 v157, 4160, v157\n\t"
      "v_add_u32 v158, 4160, v158\n\t"
      "s_cmp_eq_u32 %[ph], 4\n\t"
      "v_exp_f32 v62, v62\n\t"
      "v_exp_f32 v63, v63\n\t"
      "s_cbranch_scc1 2f\n\t"
      "s_cmp_eq_u32 %[ph], 2\n\t"
      "s_cbranch_scc1 10f\n\t"
      "s_cmp_eq_u32 %[ph], 3\n\t"
      "s_cbranch_scc1 4f\n\t"
      "s_setprio 0\n\t"
      "s_waitcnt vmcnt(0)\n\t"
      "s_mov_b64 exec, 1\n\t"
      "v_mov_b32 v160, 1\n\t"
      "ds_add_rtn_u32 v160, %[cna], v160\n\t"
      "s_waitcnt lgkmcnt(0)\n\t"
      "v_cmp_eq_u32 vcc, 7, v160\n\t"
      "s_nop 1\n\t"
      "s_cbranch_vccz 7f\n\t"
      "global_store_dwordx2 %[fo], %[tag], %[ws] sc1\n\t"
      "7:\n\t"
      "s_mov_b64 exec, -1\n\t"
      "s_nop 4\n\t"
      "s_branch 1b\n\t"
      "10:\n\t"
      "global_load_dwordx2 v[160:161], %[po], %[ws] sc1\n\t"
      "s_branch 1b\n\t"
      "4:\n\t"
      "s_mov_b32 %[cnt], 0x40000\n\t"
      "5:\n\t"
      "s_waitcnt vmcnt(0)\n\t"
      "v_cmp_ne_u64 vcc, v[160:161], %[tag]\n\t"
      "s_sub_u32 %[cnt], %[cnt], 1\n\t"
      "s_cbranch_scc1 6f\n\t"
      "s_cbranch_vccz 6f\n\t"
      "s_sleep 2\n\t"
      "global_load_dwordx2 v[160:161], %[po], %[ws] sc1\n\t"
      "s_branch 5b\n\t"
      "6:\n\t"
      "buffer_load_dwordx4 v[124:127], %[oh0], s[60:63], %[so] offen offset:0 sc1\n\t"
      "buffer_load_dwordx4 v[136:139], %[ow1], s[60:63], %[so] offen offset:0 sc1\n\t"
      "buffer_load_dwordx4 v[132:135], %[ow0], s[60:63], %[so] offen offset:0 sc1\n\t"
      "buffer_load_dwordx4 v[128:131], %[oh1], s[60:63], %[so] offen offset:0 sc1\n\t"
      "buffer_load_dwordx4 v[140:143], %[oh0], s[60:63], %[so] offen offset:1024 sc1\n\t"
      "buffer_load_dwordx4 v[152:155], %[ow1], s[60:63], %[so] offen offset:1024 sc1\n\t"
      "buffer_load_dwordx4 v[148:151], %[ow0], s[60:63], %[so] offen offset:1024 sc1\n\t"
      "buffer_load_dwordx4 v[144:147], %[oh1], s[60:63], %[so] offen offset:1024 sc1\n\t"
      "s_branch 1b\n\t"
      "2:\n\t"
      "s_cmp_lt_u32 %[wv], 4\n\t"
      "s_cbranch_scc1 9f\n\t"
      "s_setprio 2\n\t"
      "s_branch 11f\n\t"
      "9:\n\t"
      "s_setprio 1\n\t"
      "11:\n\t"
      "s_mov_b32 %[cnt], 6\n\t"
      "3:\n\t"
      "s_cmp_lg_u32 %[cnt], %[swc]\n\t"
      "s_cbranch_scc1 9f\n\t"
      "s_setprio 0\n\t"
      "9:\n\t"
      "v_exp_f32 v32, v32\n\t"
      "v_exp_f32 v33, v33\n\t"
      "v_exp_f32 v34, v34\n\t"
      "v_smfmac_f32_32x32x32_bf16 %[O1], v[112:115], v[56:63], %[idx]\n\t"
      "v_exp_f32 v35, v35\n\t"
      "v_exp_f32 v36, v36\n\t"
      "v_exp_f32 v37, v37\n\t"
      "v_exp_f32 v38, v38\n\t"
      "v_mfma_f32_32x32x16_bf16 v[48:63], v[96:99], v[120:123], v[80:95]\n\t"
      "ds_read_b128 v[96:99], v156 offset:2080\n\t"
      "ds_read_b128 v[108:111], v157 offset:2080\n\t"
      "ds_read_b128 v[112:115], v158 offset:2080\n\t"
      "v_exp_f32 v39, v39\n\t"
      "v_exp_f32 v40, v40\n\t"
      "v_exp_f32 v41, v41\n\t"
      "v_exp_f32 v42, v42\n\t"
      "v_smfmac_f32_32x32x32_bf16 %[O0], v[100:103], v[32:39], %[idx]\n\t"
      "v_exp_f32 v43, v43\n\t"
      "v_exp_f32 v44, v44\n\t"
      "v_exp_f32 v45, v45\n\t"
      "s_add_u32 %[so], %[so], 2048\n\t"
      "s_waitcnt vmcnt(5)\n\t"
      "v_mfma_f32_32x32x16_bf16 %[F00], v[132:135], v[124:127], %[F00]\n\t"
      "v_exp_f32 v46, v46\n\t"
      "v_exp_f32 v47, v47\n\t"
      "v_exp_f32 v48, v48\n\t"
      "v_exp_f32 v49, v49\n\t"
      "v_exp_f32 v50, v50\n\t"
      "v_smfmac_f32_32x32x32_bf16 %[O0], v[104:107], v[40:47], %[idx]\n\t"
      "v_exp_f32 v51, v51\n\t"
      "v_exp_f32 v52, v52\n\t"
      "v_exp_f32 v53, v53\n\t"
      "v_exp_f32 v54, v54\n\t"
      "s_waitcnt lgkmcnt(0)\n\t"
      "v_mfma_f32_32x32x16_bf16 v[32:47], v[96:99], v[116:119], v[64:79]\n\t"
      "v_exp_f32 v55, v55\n\t"
      "v_exp_f32 v56, v56\n\t"
      "v_exp_f32 v57, v57\n\t"
      "v_exp_f32 v58, v58\n\t"
      "v_smfmac_f32_32x32x32_bf16 %[O1], v[100:103], v[48:55], %[idx]\n\t"
      "v_exp_f32 v59, v59\n\t"
      "v_exp_f32 v60, v60\n\t"
      "v_exp_f32 v61, v61\n\t"
      "v_mfma_f32_32x32x16_bf16 %[F01], v[136:139], v[124:127], %[F01]\n\t"
      "buffer_load_dwordx4 v[124:127], %[oh0], s[60:63], %[so] offen offset:0 sc1\n\t"
      "v_exp_f32 v62, v62\n\t"
      "v_exp_f32 v63, v63\n\t"
      "v_exp_f32 v32, v32\n\t"
      "v_exp_f32 v33, v33\n\t"
      "v_exp_f32 v34, v34\n\t"
      "v_smfmac_f32_32x32x32_bf16 %[O1], v[104:107], v[56:63], %[idx]\n\t"
      "v_exp_f32 v35, v35\n\t"
      "v_exp_f32 v36, v36\n\t"
      "v_exp_f32 v37, v37\n\t"
      "v_exp_f32 v38, v38\n\t"
      "v_mfma_f32_32x32x16_bf16 v[48:63], v[96:99], v[120:123], v[80:95]\n\t"
      "ds_read_b128 v[96:99], v156 offset:4160\n\t"
      "ds_read_b128 v[100:103], v157 offset:4160\n\t"
      "ds_read_b128 v[104:107], v158 offset:4160\n\t"
      "v_exp_f32 v39, v39\n\t"
      "v_exp_f32 v40, v40\n\t"
      "v_exp_f32 v41, v41\n\t"
      "v_exp_f32 v42, v42\n\t"
      "v_smfmac_f32_32x32x32_bf16 %[O0], v[108:111], v[32:39], %[idx]\n\t"
      "v_exp_f32 v43, v43\n\t"
      "v_exp_f32 v44, v44\n\t"
      "v_exp_f32 v45, v45\n\t"
      "s_waitcnt vmcnt(5)\n\t"
      "v_mfma_f32_32x32x16_bf16 %[F11], v[136:139], v[128:131], %[F11]\n\t"
      "buffer_load_dwordx4 v[136:139], %[ow1], s[60:63], %[so] offen offset:0 sc1\n\t"
      "v_exp_f32 v46, v46\n\t"
      "v_exp_f32 v47, v47\n\t"
      "v_exp_f32 v48, v48\n\t"
      "v_exp_f32 v49, v49\n\t"
      "v_exp_f32 v50, v50\n\t"
      "v_smfmac_f32_32x32x32_bf16 %[O0], v[112:115], v[40:47], %[idx]\n\t"
      "v_exp_f32 v51, v51\n\t"
      "v_exp_f32 v52, v52\n\t"
      "v_exp_f32 v53, v53\n\t"
      "v_exp_f32 v54, v54\n\t"
      "s_waitcnt lgkmcnt(0)\n\t"
      "v_mfma_f32_32x32x16_bf16 v[32:47], v[96:99], v[116:119], v[64:79]\n\t"
      "v_exp_f32 v55, v55\n\t"
      "v_exp_f32 v56, v56\n\t"
      "v_exp_f32 v57, v57\n\t"
      "v_exp_f32 v58, v58\n\t"
      "v_smfmac_f32_32x32x32_bf16 %[O1], v[108:111], v[48:55], %[idx]\n\t"
      "v_exp_f32 v59, v59\n\t"
      "v_exp_f32 v60, v60\n\t"
      "v_exp_f32 v61, v61\n\t"
      "v_mfma_f32_32x32x16_bf16 %[F10], v[132:135], v[128:131], %[F10]\n\t"
      "buffer_load_dwordx4 v[132:135], %[ow0], s[60:63], %[so] offen offset:0 sc1\n\t"
      "buffer_load_dwordx4 v[128:131], %[oh1], s[60:63], %[so] offen offset:0 sc1\n\t"
      "v_exp_f32 v62, v62\n\t"
      "v_exp_f32 v63, v63\n\t"
      "v_exp_f32 v32, v32\n\t"
      "v_exp_f32 v33, v33\n\t"
      "v_exp_f32 v34, v34\n\t"
      "v_smfmac_f32_32x32x32_bf16 %[O1], v[112:115], v[56:63], %[idx]\n\t"
      "v_exp_f32 v35, v35\n\t"
      "v_exp_f32 v36, v36\n\t"
      "v_exp_f32 v37, v37\n\t"
      "v_exp_f32 v38, v38\n\t"
      "v_mfma_f32_32x32x16_bf16 v[48:63], v[96:99], v[120:123], v[80:95]\n\t"
      "ds_read_b128 v[96:99], v156 offset:6240\n\t"
      "ds_read_b128 v[108:111], v157 offset:6240\n\t"
      "ds_read_b128 v[112:115], v158 offset:6240\n\t"
      "v_exp_f32 v39, v39\n\t"
      "v_exp_f32 v40, v40\n\t"
      "v_exp_f32 v41, v41\n\t"
      "v_exp_f32 v42, v42\n\t"
      "v_smfmac_f32_32x32x32_bf16 %[O0], v[100:103], v[32:39], %[idx]\n\t"
      "v_exp_f32 v43, v43\n\t"
      "v_exp_f32 v44, v44\n\t"
      "v_exp_f32 v45, v45\n\t"
      "s_waitcnt vmcnt(5)\n\t"
      "v_mfma_f32_32x32x16_bf16 %[F00], v[148:151], v[140:143], %[F00]\n\t"
      "v_exp_f32 v46, v46\n\t"
      "v_exp_f32 v47, v47\n\t"
      "v_exp_f32 v48, v48\n\t"
      "v_exp_f32 v49, v49\n\t"
      "v_exp_f32 v50, v50\n\t"
      "v_smfmac_f32_32x32x32_bf16 %[O0], v[104:107], v[40:47], %[idx]\n\t"
      "v_exp_f32 v51, v51\n\t"
      "v_exp_f32 v52, v52\n\t"
      "v_exp_f32 v53, v53\n\t"
      "v_exp_f32 v54, v54\n\t"
      "s_waitcnt lgkmcnt(0)\n\t"
      "v_mfma_f32_32x32x16_bf16 v[32:47], v[96:99], v[116:119], v[64:79]\n\t"
      "v_exp_f32 v55, v55\n\t"
      "v_exp_f32 v56, v56\n\t"
      "v_exp_f32 v57, v57\n\t"
      "v_exp_f32 v58, v58\n\t"
      "v_smfmac_f32_32x32x32_bf16 %[O1], v[100:103], v[48:55], %[idx]\n\t"
      "v_exp_f32 v59, v59\n\t"
      "v_exp_f32 v60, v60\n\t"
      "v_exp_f32 v61, v61\n\t"
      "v_mfma_f32_32x32x16_bf16 %[F01], v[152:155], v[140:143], %[F01]\n\t"
      "buffer_load_dwordx4 v[140:143], %[oh0], s[60:63], %[so] offen offset:1024 sc1\n\t"
      "v_exp_f32 v62, v62\n\t"
      "v_exp_f32 v63, v63\n\t"
      "v_exp_f32 v32, v32\n\t"
      "v_exp_f32 v33, v33\n\t"
      "v_exp_f32 v34, v34\n\t"
      "v_smfmac_f32_32x32x32_bf16 %[O1], v[104:107], v[56:63], %[idx]\n\t"
      "v_exp_f32 v35, v35\n\t"
      "v_exp_f32 v36, v36\n\t"
      "v_exp_f32 v37, v37\n\t"
      "v_exp_f32 v38, v38\n\t"
      "v_mfma_f32_32x32x16_bf16 v[48:63], v[96:99], v[120:123], v[80:95]\n\t"
      "ds_read_b128 v[96:99], v156 offset:8320\n\t"
      "ds_read_b128 v[100:103], v157 offset:8320\n\t"
      "ds_read_b128 v[104:107], v158 offset:8320\n\t"
      "v_exp_f32 v39, v39\n\t"
      "v_exp_f32 v40, v40\n\t"
      "v_exp_f32 v41, v41\n\t"
      "v_exp_f32 v42, v42\n\t"
      "v_smfmac_f32_32x32x32_bf16 %[O0], v[108:111], v[32:39], %[idx]\n\t"
      "v_exp_f32 v43, v43\n\t"
      "v_exp_f32 v44, v44\n\t"
      "v_exp_f32 v45, v45\n\t"
      "s_waitcnt vmcnt(5)\n\t"
      "v_mfma_f32_32x32x16_bf16 %[F11], v[152:155], v[144:147], %[F11]\n\t"
      "buffer_load_dwordx4 v[152:155], %[ow1], s[60:63], %[so] offen offset:1024 sc1\n\t"
      "s_sub_u32 %[cnt], %[cnt], 1\n\t"
      "v_exp_f32 v46, v46\n\t"
      "v_exp_f32 v47, v47\n\t"
      "v_exp_f32 v48, v48\n\t"
      "v_exp_f32 v49, v49\n\t"
      "v_exp_f32 v50, v50\n\t"
      "v_smfmac_f32_32x32x32_bf16 %[O0], v[112:115], v[40:47], %[idx]\n\t"
      "v_exp_f32 v51, v51\n\t"
      "v_exp_f32 v52, v52\n\t"
      "v_exp_f32 v53, v53\n\t"
      "v_exp_f32 v54, v54\n\t"
      "s_waitcnt lgkmcnt(0)\n\t"
      "v_mfma_f32_32x32x16_bf16 v[32:47], v[96:99], v[116:119], v[64:79]\n\t"
      "v_exp_f32 v55, v55\n\t"
      "v_exp_f32 v56, v56\n\t"
      "v_exp_f32 v57, v57\n\t"
      "v_exp_f32 v58, v58\n\t"
      "v_smfmac_f32_32x32x32_bf16 %[O1], v[108:111], v[48:55], %[idx]\n\t"
      "v_exp_f32 v59, v59\n\t"
      "v_exp_f32 v60, v60\n\t"
      "v_exp_f32 v61, v61\n\t"
      "v_mfma_f32_32x32x16_bf16 %[F10], v[148:151], v[144:147], %[F10]\n\t"
      "buffer_load_dwordx4 v[148:151], %[ow0], s[60:63], %[so] offen offset:1024 sc1\n\t"
      "buffer_load_dwordx4 v[144:147], %[oh1], s[60:63], %[so] offen offset:1024 sc1\n\t"
      "v_add_u32 v156, 8320, v156\n\t"
      "v_add_u32 v157, 8320, v157\n\t"
      "v_add_u32 v158, 8320, v158\n\t"
      "s_cmp_lg_u32 %[cnt], 0\n\t"
      "v_exp_f32 v62, v62\n\t"
      "v_exp_f32 v63, v63\n\t"
      "s_cbranch_scc1 3b\n\t"
      "s_nop 1\n\t"
      "v_smfmac_f32_32x32x32_bf16 %[O1], v[112:115], v[56:63], %[idx]\n\t"
      "s_waitcnt vmcnt(4)\n\t"
      "v_mfma_f32_32x32x16_bf16 %[F00], v[132:135], v[124:127], %[F00]\n\t"
      "v_mfma_f32_32x32x16_bf16 %[F01], v[136:139], v[124:127], %[F01]\n\t"
      "v_mfma_f32_32x32x16_bf16 %[F11], v[136:139], v[128:131], %[F11]\n\t"
      "v_mfma_f32_32x32x16_bf16 %[F10], v[132:135], v[128:131], %[F10]\n\t"
      "s_waitcnt vmcnt(0)\n\t"
      "v_mfma_f32_32x32x16_bf16 %[F00], v[148:151], v[140:143], %[F00]\n\t"
      "v_mfma_f32_32x32x16_bf16 %[F01], v[152:155], v[140:143], %[F01]\n\t"
      "v_mfma_f32_32x32x16_bf16 %[F11], v[152:155], v[144:147], %[F11]\n\t"
      "v_mfma_f32_32x32x16_bf16 %[F10], v[148:151], v[144:147], %[F10]\n\t"
      "s_setprio 0\n\t"
      "s_waitcnt lgkmcnt(0)\n\t"
      "s_nop 15\n\t"
      : [O0] "=&v"(O0), [O1] "=&v"(O1), [F00] "=&v"(F00), [F01] "=&v"(F01), [F10] "=&v"(F10), [F11] "=&v"(F11), [so] "=&s"(so), [cnt] "=&s"(cnt), [ph] "=&s"(ph),
        "+{v156}"(ka_), "+{v157}"(va0_), "+{v158}"(va1_)
      : [idx] "v"(idx), [qa] "v"(QSLOT + qp * 2048 + l * 16), [k0] "v"(l * 16), [cna] "v"(CNT),
        [oh0] "v"(oh0), [oh1] "v"(oh0 + 112 * 1024), [ow0] "v"(ow0), [ow1] "v"(ow0 + 144 * 1024), [ws] "s"(ws), [wsz] "i"(WS_END),
        [wv] "s"(w), [fo] "v"(WS_FH + myflag), [tag] "v"(tag), [po] "v"(pollh), [swc] "s"(kh ? 2 : 99)
      : "v32", "v33", "v34", "v35", "v36", "v37", "v38", "v39", "v40", "v41", "v42", "v43", "v44", "v45", "v46", "v47", "v48", "v49", "v50", "v51", "v52", "v53", "v54", "v55", "v56", "v57", "v58", "v59", "v60", "v61", "v62", "v63", "v64", "v65", "v66", "v67", "v68", "v69", "v70", "v71", "v72", "v73", "v74", "v75", "v76", "v77", "v78", "v79", "v80", "v81", "v82", "v83", "v84", "v85", "v86", "v87", "v88", "v89", "v90", "v91", "v92", "v93", "v94", "v95", "v96", "v97", "v98", "v99", "v100", "v101", "v102", "v103", "v104", "v105", "v106", "v107", "v108", "v109", "v110", "v111", "v112", "v113", "v114", "v115", "v116", "v117", "v118", "v119", "v120", "v121", "v122", "v123", "v124", "v125", "v126", "v127", "v128", "v129", "v130", "v131", "v132", "v133", "v134", "v135", "v136", "v137", "v138", "v139", "v140", "v141", "v142", "v143", "v144", "v145", "v146", "v147", "v148", "v149", "v150", "v151", "v152", "v153", "v154", "v155", "v160", "v161", "s60", "s61", "s62", "s63", "vcc", "scc", "memory");
  }
  RELANE();
  STAMP(7);
  auto put_f = [&]() {
    ldsp dst = lds + w * 16384 + l * 16;
#pragma unroll
    for (int g = 0; g < 4; ++g) {
      *(LAS f32x4*)(dst + (0 * 4 + g) * 1024) = f32x4{F00[4 * g], F00[4 * g + 1], F00[4 * g + 2], F00[4 * g + 3]};
      *(LAS f32x4*)(dst + (1 * 4 + g) * 1024) = f32x4{F01[4 * g], F01[4 * g + 1], F01[4 * g + 2], F01[4 * g + 3]};
      *(LAS f32x4*)(dst + (2 * 4 + g) * 1024) = f32x4{F10[4 * g], F10[4 * g + 1], F10[4 * g + 2], F10[4 * g + 3]};
      *(LAS f32x4*)(dst + (3 * 4 + g) * 1024) = f32x4{F11[4 * g], F11[4 * g + 1], F11[4 * g + 2], F11[4 * g + 3]};
    }
  };
  const int tt = qp >> 1;
  ldsp ex = lds + EXB + qp * 4096 + l * 16;
  LAS float* exl = (LAS float*)(lds + EXL) + qp * 128 + l;
  if (kh == 0) {
    const int kq = qp & 1;
    *(LAS f32x4*)ex = f32x4{O0[0], O0[1], O0[2], O0[3]};          *(LAS f32x4*)(ex + 1024) = f32x4{O0[4], O0[5], O0[6], O0[7]};
    *(LAS f32x4*)(ex + 2048) = f32x4{O1[0], O1[1], O1[2], O1[3]}; *(LAS f32x4*)(ex + 3072) = f32x4{O1[4], O1[5], O1[6], O1[7]};
    exl[0] = h ? O0[12] : O0[8]; exl[64] = h ? O1[12] : O1[8];
    if (l == 0) __hip_atomic_fetch_add(cnt_h + 1, 1u, __ATOMIC_RELAXED, __HIP_MEMORY_SCOPE_WORKGROUP);
#pragma unroll 1
    for (unsigned spin = 0; spin < (1u << 22); ++spin) {
      const unsigned a = __hip_atomic_load(cnt_h, __ATOMIC_RELAXED, __HIP_MEMORY_SCOPE_WORKGROUP), b = __hip_atomic_load(cnt_h + 1, __ATOMIC_RELAXED, __HIP_MEMORY_SCOPE_WORKGROUP);
      if (__all(a == 8 && b == 4)) break;
      __builtin_amdgcn_s_sleep(2);
    }
    asm volatile("" ::: "memory");
    put_f();
    __builtin_amdgcn_sched_barrier(0);
    u32x4 wf[2][16];
#pragma unroll
    for (int n = 0; n < 2; ++n)
#pragma unroll
      for (int s = 0; s < 16; ++s) wf[n][s] = LD_DEV(WS_W + ((2 * nb + n) * 144 + 112 + 16 * kq + s) * 1024 + l * 16);
    if (blockIdx.x == 0 && w == 0) {
      const u64 f0 = ld_flag(ws, WS_FQ + l * 64), f1 = ld_flag(ws, WS_FQ + (64 + l) * 64), f2 = ld_flag(ws, WS_FQ + (128 + l) * 64), f3 = ld_flag(ws, WS_FQ + (192 + l) * 64);
      poll_flags(ws, WS_FQ + l * 64, tag, f0); poll_flags(ws, WS_FQ + (64 + l) * 64, tag, f1);
      poll_flags(ws, WS_FQ + (128 + l) * 64, tag, f2); poll_flags(ws, WS_FQ + (192 + l) * 64, tag, f3);
      if (l == 0) st_flag(ws, WS_EPOCH, tag);
    }
    asm volatile("s_waitcnt lgkmcnt(0)\n\ts_barrier" ::: "memory");
    STAMP(8);
    asm volatile("s_barrier" ::: "memory");
    STAMP(9);
    const unsigned ooff = WS_O + ((2 * tb + tt) * 32 + 16 * kq) * 1024 + l * 16;
    f32x16 S0, S1;
#pragma unroll 1
    for (unsigned spin = 0; spin < (1u << 16); ++spin) {
      u32x4 of[16];
#pragma unroll
      for (int s = 0; s < 16; ++s) of[s] = LD_DEV(ooff + s * 1024);
      __builtin_amdgcn_sched_barrier(0);
      S0 = f32x16{}; S1 = f32x16{};
      unsigned mx = 0;
#pragma unroll
      for (int s = 0; s < 16; ++s) {
        S0 = MFMA32(as_bf(wf[0][s]), as_bf(of[s]), S0); S1 = MFMA32(as_bf(wf[1][s]), as_bf(of[s]), S1);
        mx = max(max(max(mx, of[s][0]), max(of[s][1], of[s][2])), of[s][3]);
        PIN(mx);
        __builtin_amdgcn_sched_barrier(0);
      }
      if (__all(mx != 0xffffffffu)) break;
      asm volatile("" ::: "memory");
    }
    STAMP(10);
    ldsp dst = lds + SPART + qp * 8192 + l * 16;
#pragma unroll
    for (int g = 0; g < 4; ++g) {
      *(LAS f32x4*)(dst + g * 1024) = f32x4{S0[4 * g], S0[4 * g + 1], S0[4 * g + 2], S0[4 * g + 3]};
      *(LAS f32x4*)(dst + 4096 + g * 1024) = f32x4{S1[4 * g], S1[4 * g + 1], S1[4 * g + 2], S1[4 * g + 3]};
    }
    asm volatile("s_waitcnt lgkmcnt(0)\n\ts_barrier" ::: "memory");
    STAMP(11);
  } else {
    const int nt = qp & 1;
    const int token = tb * 64 + tt * 32 + t, ncol = nb * 64 + nt * 32 + 4 * h;
    asm volatile("s_barrier" ::: "memory");
    STAMP(8);
#pragma unroll
    for (int j = 0; j < 2; ++j) {
      const f32x16& Oj = j ? O1 : O0;
      const int qt = qb * 8 + 2 * qp + j;
      float o8[8], lsum = (h ? Oj[12] : Oj[8]) + exl[64 * j];
      const f32x4 a = *(LAS f32x4*)(ex + 2048 * j), b = *(LAS f32x4*)(ex + 2048 * j + 1024);
#pragma unroll
      for (int e = 0; e < 4; ++e) { o8[e] = Oj[e] + a[e]; o8[4 + e] = Oj[4 + e] + b[e]; }
      bool ok = lsum <= 1.8e19f;
#pragma unroll
      for (int r = 0; r < 8; ++r) ok = ok && (fabsf(o8[r]) <= 3.0e38f);
      if (__builtin_expect(!__all(ok), 0)) {
        const u32x4 qf = LD_DEV(WS_Q + (head * 64 + qt) * 1024 + l * 16);
        const unsigned cv = (t == 16 || t == 28) ? 0x3f803f80u : 0u;
        float mrun = -3.0e38f; f32x16 O = {};
#pragma unroll 1
        for (int kt = 0; kt < 64; ++kt) {
          const unsigned kvo = WS_KV + (head * 64 + kt) * 2048;
          f32x16 zero = {};
          f32x16 S = MFMA32(as_bf(LD_DEV(kvo + l * 16)), as_bf(qf), zero);
          u32x4 v0 = LD_DEV(kvo + 1024 + h * 256 + (t & 15) * 16), v1 = LD_DEV(kvo + 1536 + h * 256 + (t & 15) * 16);
          if (t >= 16) { v0 = u32x4{cv, cv, cv, cv}; v1 = v0; }
          const float mn = fmaxf(mrun, max16(S));
          const float al = __builtin_amdgcn_exp2f(mrun - mn); mrun = mn;
#pragma unroll
          for (int r = 0; r < 16; ++r) { O[r] *= al; S[r] = __builtin_amdgcn_exp2f(S[r] - mn); }
          O = smf(v0, lo8(S), O, idx);
          O = smf(v1, hi8(S), O, idx);
        }
        lsum = h ? O[12] : O[8];
#pragma unroll
        for (int r = 0; r < 8; ++r) o8[r] = O[r];
      }
      const float inv = __builtin_amdgcn_rcpf(lsum);
      u32x4 ov = {pk2(o8[0] * inv, o8[1] * inv), pk2(o8[2] * inv, o8[3] * inv), pk2(o8[4] * inv, o8[5] * inv), pk2(o8[6] * inv, o8[7] * inv)};
#pragma unroll
      for (int e = 0; e < 4; ++e) ov[e] = min(ov[e], 0xfffffffeu);
      ST_DEV(ov, WS_O + (qt * 32 + head) * 1024 + l * 16);
    }
    __builtin_amdgcn_sched_barrier(0);
    u32x2 bb[4], xv[4];
#pragma unroll
    for (int g = 0; g < 4; ++g) { bb[g] = *(const u32x2*)(b2 + (ncol + 8 * g) * 2); xv[g] = *(const u32x2*)(x + (size_t)token * 1024 + (ncol + 8 * g) * 2); }
    put_f();
    asm volatile("s_waitcnt lgkmcnt(0)\n\ts_barrier" ::: "memory");
    STAMP(9);
    f32x4 gv[4];
#pragma unroll
    for (int g = 0; g < 4; ++g) {
      ldsp src = lds + (qp * 4 + g) * 1024 + l * 16;
      f32x4 a = {bf_lo(bb[g][0]), bf_hi(bb[g][0]), bf_lo(bb[g][1]), bf_hi(bb[g][1])};
#pragma unroll
      for (int s = 0; s < 8; ++s) a += *(LAS f32x4*)(src + s * 16384);
#pragma unroll
      for (int e = 0; e < 4; ++e) gv[g][e] = a[e] * __builtin_amdgcn_rcpf(1.0f + __builtin_amdgcn_exp2f(a[e] * -1.4426950408889634f));
      PIN(gv[g]);
    }
    STAMP(10);
    asm volatile("s_barrier" ::: "memory");
    STAMP(11);
    ldsp src = lds + SPART + tt * 16384 + nt * 4096 + l * 16;
#pragma unroll
    for (int g = 0; g < 4; ++g) {
      const f32x4 a = *(LAS f32x4*)(src + g * 1024) + *(LAS f32x4*)(src + 8192 + g * 1024);
      const u32x2 r0 = {pk2(fmaf(a[0], gv[g][0], bf_lo(xv[g][0])), fmaf(a[1], gv[g][1], bf_hi(xv[g][0]))),
                        pk2(fmaf(a[2], gv[g][2], bf_lo(xv[g][1])), fmaf(a[3], gv[g][3], bf_hi(xv[g][1])))};
      *(u32x2*)(out + (size_t)token * 1024 + (ncol + 8 * g) * 2) = r0;
    }
  }
  STAMP(12);
  STAMP_FLUSH();
}

extern "C" void kernel_launch(void* const* d_in, const int* in_sizes, int n_in, void* d_out, int out_size, void* d_ws, size_t ws_size, hipStream_t stream) {
  (void)hipFuncSetAttribute((const void*)fused_block, hipFuncAttributeMaxDynamicSharedMemorySize, LDS_BYTES);
  fused_block<<<256, 512, LDS_BYTES, stream>>>((const char*)d_in[0], (const char*)d_in[1], (const char*)d_in[2], (const char*)d_in[4], (const char*)d_in[5],
                                               (const char*)d_in[6], (const char*)d_in[7], (char*)d_ws, (char*)d_out);
}
```
